```python
import torch

def _softmax(x, axis=-1):
    return torch.softmax(x, dim=axis)
def _gelu(x):
    return torch.nn.functional.gelu(x, approximate="tanh")
def _silu(x):
    return torch.nn.functional.silu(x)
def _sigmoid(x):
    return torch.sigmoid(x)
def _layernorm(x, axis=-1, eps=1e-5):
    m = x.mean(dim=axis, keepdim=True)
    v = x.var(dim=axis, keepdim=True, unbiased=False)
    return (x - m) / torch.sqrt(v + eps)
def _scatter_add(tgt, idx, src, axis=0):
    return tgt.clone().index_add_(axis, idx, src)
def _segment_sum(src, seg, n):
    out = torch.zeros((n,) + src.shape[1:], dtype=src.dtype)
    return out.index_add_(0, seg, src)
def _pad(x, pw):
    flat = tuple(v for pair in reversed(pw) for v in pair)
    return torch.nn.functional.pad(x, flat)

kernel_name = 'parallel_sum__ssm_conv_s4d__dense_activation_gelu__prompt'

def setup_inputs():

    _w = (lambda a: (lambda t: t.to(torch.bfloat16) if t.is_floating_point() else t)(torch.as_tensor(a)))
    def _A_setup():
        Bsz, L, D, N = 1, 2048, 512, 128
        return {
            "u": _w((lambda *s: torch.randn(*s, dtype=torch.bfloat16) * 0.05)(Bsz, L, D)),
            "log_A_real": _w((lambda *s: torch.randn(*s, dtype=torch.bfloat16) * 12.8)(D, N) * 0.5),
            "A_imag": _w((lambda *s: torch.randn(*s, dtype=torch.bfloat16) * 0.05)(D, N) * 3.14),
            "C_real": _w((lambda *s: torch.randn(*s, dtype=torch.bfloat16) * 0.1)(D, N) / N ** 0.5),
            "C_imag": _w((lambda *s: torch.randn(*s, dtype=torch.bfloat16) * 0.2)(D, N) / N ** 0.5),
            "log_dt": _w((lambda *s: torch.randn(*s, dtype=torch.bfloat16) * 0.05)(D) * 0.5 - 4.0),
            "D_skip": _w((lambda *s: torch.randn(*s, dtype=torch.bfloat16) * 0.05)(D)),
        }
    _A_in = _A_setup()

    def _B_setup():
        B, S, D, F = 1, 2048, 512, 1792
        return {
            "x": _w((lambda *s: torch.randn(*s, dtype=torch.bfloat16) * 0.05)(B, S, D)),
            "w1": _w((lambda *s: torch.randn(*s, dtype=torch.bfloat16) * 0.8)(D, F) * (D ** -0.5)),
            "b1": _w((lambda *s: torch.randn(*s, dtype=torch.bfloat16) * 12.8)(F) * 0.01),
            "w2": _w((lambda *s: torch.randn(*s, dtype=torch.bfloat16) * 0.8)(F, D) * (F ** -0.5)),
            "b2": _w((lambda *s: torch.randn(*s, dtype=torch.bfloat16) * 12.8)(D) * 0.01),
        }
    _B_in = _B_setup()

    return {
        "A_u": _A_in["u"],
        "A_log_A_real": _A_in["log_A_real"],
        "A_A_imag": _A_in["A_imag"],
        "A_C_real": _A_in["C_real"],
        "A_C_imag": _A_in["C_imag"],
        "A_log_dt": _A_in["log_dt"],
        "A_D_skip": _A_in["D_skip"],
        "B_x": _B_in["x"],
        "B_w1": _B_in["w1"],
        "B_b1": _B_in["b1"],
        "B_w2": _B_in["w2"],
        "B_b2": _B_in["b2"],
    }

def reference(A_u, A_log_A_real, A_A_imag, A_C_real, A_C_imag, A_log_dt, A_D_skip, B_x, B_w1, B_b1, B_w2, B_b2):
    def _A_ref(u, log_A_real, A_imag, C_real, C_imag, log_dt, D_skip):
        L = u.shape[1]
        dt = torch.exp(log_dt)
        A = -torch.exp(log_A_real) + 1j * A_imag
        C = (C_real + 1j * C_imag) * dt[:, None]
        pos = (lambda x, dt: x.to(dt))(torch.arange(L), log_dt.dtype)
        P = torch.exp((A * dt[:, None])[:, :, None] * pos[None, None, :])
        K = 2.0 * torch.einsum("dn,dnl->dl", C, P).real
        L2 = 2 * L
        u_t = (lambda x, *d: x.permute(*d))(u, 0, 2, 1)
        Kf = (lambda x, *a, **k: torch.fft.rfft(x.float(), *a, **k))(K, L2)
        Uf = (lambda x, *a, **k: torch.fft.rfft(x.float(), *a, **k))(u_t, L2)
        y = (lambda x, *a, **k: torch.fft.irfft(x, *a, **k).to(torch.bfloat16))(Uf * Kf[None, :, :], L2)[:, :, :L]
        y = (lambda x, *d: x.permute(*d))(y, 0, 2, 1)
        return y + u * D_skip[None, None, :]

    def _B_ref(x, w1, b1, w2, b2):
        h = torch.einsum("bsd,df->bsf", x, w1) + b1
        h = _gelu(h)
        return torch.einsum("bsf,fd->bsd", h, w2) + b2

    x = A_u
    xn = _layernorm(x, axis=-1)
    return x + _A_ref(xn, A_log_A_real, A_A_imag, A_C_real, A_C_imag, A_log_dt, A_D_skip) + _B_ref(xn, B_w1, B_b1, B_w2, B_b2)
```

```cpp
#include <hip/hip_runtime.h>
#include <hip/hip_bf16.h>
#include <stdint.h>

typedef unsigned short u16;
__device__ __forceinline__ float bf2f(u16 v){ return __uint_as_float(((unsigned)v)<<16); }
__device__ __forceinline__ u16 f2bf(float f){ unsigned u=__float_as_uint(f); u = (u + 0x7FFF + ((u>>16)&1))>>16; return (u16)u; }
__device__ __forceinline__ float rbf(float f){ return bf2f(f2bf(f)); }

constexpr int L=2048, D=512, N=128, F=1792;

__global__ void ln_k(const u16* x, u16* xn){
  int row=blockIdx.x; int t=threadIdx.x;
  float v[8]; float s=0;
  for(int i=0;i<8;i++){ v[i]=bf2f(x[row*D+t*8+i]); s+=v[i]; }
  for(int o=32;o;o>>=1) s+=__shfl_xor(s,o);
  float mean=s/D; float q=0;
  for(int i=0;i<8;i++){ float d=v[i]-mean; q+=d*d; }
  for(int o=32;o;o>>=1) q+=__shfl_xor(q,o);
  float var=q/D;
  float m=rbf(mean), vv=rbf(var);
  float den=rbf(sqrtf(rbf(vv+1e-5f)));
  for(int i=0;i<8;i++){ xn[row*D+t*8+i]=f2bf(rbf(v[i]-m)/den); }
}
__global__ void kgen_k(const u16* lar,const u16* aim,const u16* cr,const u16* ci,const u16* ldt,float* K, int roundpos){
  int d=blockIdx.y; int l=blockIdx.x*256+threadIdx.x;
  float dt=rbf(expf(bf2f(ldt[d])));
  float p = roundpos? rbf((float)l) : (float)l;
  float acc=0;
  for(int n=0;n<N;n++){
    float ar=-rbf(expf(bf2f(lar[d*N+n])))*dt;
    float ai=bf2f(aim[d*N+n])*dt;
    float c_r=bf2f(cr[d*N+n])*dt, c_i=bf2f(ci[d*N+n])*dt;
    float e=expf(ar*p); float sn,cs; sincosf(ai*p,&sn,&cs);
    acc += c_r*(e*cs) - c_i*(e*sn);
  }
  K[d*L+l]=2.f*acc;
}
__global__ void conv_k(const float* K,const u16* xn,const u16* dsk,const u16* x,u16* t){
  int d=blockIdx.x*64+threadIdx.x; int l=blockIdx.y;
  float acc=0;
  for(int j=0;j<=l;j++) acc += K[d*L+(l-j)]*bf2f(xn[j*D+d]);
  float y=rbf(acc);
  float ya=rbf(y+rbf(bf2f(xn[l*D+d])*bf2f(dsk[d])));
  t[l*D+d]=f2bf(bf2f(x[l*D+d])+ya);
}
__global__ void g1_k(const u16* xn,const u16* w1,const u16* b1,u16* h){
  int f=blockIdx.x*64+threadIdx.x; int l=blockIdx.y;
  float acc=0;
  for(int k=0;k<D;k++) acc+=bf2f(xn[l*D+k])*bf2f(w1[k*F+f]);
  float v=rbf(rbf(acc)+bf2f(b1[f]));
  float g=0.5f*v*(1.f+tanhf(0.7978845608028654f*(v+0.044715f*v*v*v)));
  h[l*F+f]=f2bf(g);
}
__global__ void g2_k(const u16* h,const u16* w2,const u16* b2,const u16* t,u16* out){
  int d=blockIdx.x*64+threadIdx.x; int l=blockIdx.y;
  float acc=0;
  for(int k=0;k<F;k++) acc+=bf2f(h[l*F+k])*bf2f(w2[k*D+d]);
  float v=rbf(rbf(acc)+bf2f(b2[d]));
  out[l*D+d]=f2bf(bf2f(t[l*D+d])+v);
}
#define ROUNDPOS 1
extern "C" void kernel_launch(void* const* d_in, const int* in_sizes, int n_in,
                              void* d_out, int out_size, void* d_ws, size_t ws_size,
                              hipStream_t stream) {
  const u16* x=(const u16*)d_in[0]; const u16* lar=(const u16*)d_in[1]; const u16* aim=(const u16*)d_in[2];
  const u16* cr=(const u16*)d_in[3]; const u16* ci=(const u16*)d_in[4]; const u16* ldt=(const u16*)d_in[5];
  const u16* dsk=(const u16*)d_in[6]; const u16* w1=(const u16*)d_in[8]; const u16* b1=(const u16*)d_in[9];
  const u16* w2=(const u16*)d_in[10]; const u16* b2=(const u16*)d_in[11];
  char* ws=(char*)d_ws;
  u16* xn=(u16*)ws; float* K=(float*)(ws+(4<<20)); u16* t=(u16*)(ws+(16<<20)); u16* h=(u16*)(ws+(32<<20));
  ln_k<<<L,64,0,stream>>>(x,xn);
  kgen_k<<<dim3(L/256,D),256,0,stream>>>(lar,aim,cr,ci,ldt,K,ROUNDPOS);
  conv_k<<<dim3(D/64,L),64,0,stream>>>(K,xn,dsk,x,t);
  g1_k<<<dim3(F/64,L),64,0,stream>>>(xn,w1,b1,h);
  g2_k<<<dim3(D/64,L),64,0,stream>>>(h,w2,b2,t,(u16*)d_out);
}
```

```cpp
#include <hip/hip_runtime.h>
#include <stdint.h>

typedef unsigned short u16;
typedef __attribute__((ext_vector_type(8))) __bf16 bf16x8;
typedef __attribute__((ext_vector_type(8))) _Float16 f16x8;
typedef __attribute__((ext_vector_type(4))) float f32x4;
typedef __attribute__((ext_vector_type(4))) unsigned u32x4;
typedef __attribute__((ext_vector_type(2))) unsigned u32x2;

constexpr int L = 2048, D = 512, NS = 128, F = 1792;
constexpr int XR = 2304;

__device__ __forceinline__ float bf2f(unsigned v) { return __uint_as_float(v << 16); }
__device__ __forceinline__ unsigned f2bf(float f) {
  __bf16 b = (__bf16)f; return (unsigned)__builtin_bit_cast(u16, b);
}
__device__ __forceinline__ float rbf(float f) { return bf2f(f2bf(f)); }
__device__ __forceinline__ unsigned pk2(float a, float b) { return f2bf(a) | (f2bf(b) << 16); }
__device__ __forceinline__ unsigned f2h(float f) { _Float16 h = (_Float16)f; return (unsigned)__builtin_bit_cast(u16, h); }
__device__ __forceinline__ float h2f(unsigned v) { return (float)__builtin_bit_cast(_Float16, (u16)v); }

constexpr size_t WS_XN  = 0;
constexpr size_t WS_XRT = WS_XN + (size_t)L * D * 2;
constexpr size_t WS_W1T = WS_XRT + (size_t)D * XR * 2;
constexpr size_t WS_W2T = WS_W1T + (size_t)F * D * 2;
constexpr size_t WS_KF  = WS_W2T + (size_t)F * D * 2;
constexpr size_t WS_KINV = WS_KF + (size_t)D * L * 2;
constexpr size_t WS_H   = WS_KINV + 4096;
constexpr size_t WS_YAT = WS_H + (size_t)L * F * 2;

struct cplx { float r, i; };
__device__ __forceinline__ cplx cmul(cplx a, cplx b) { return {a.r * b.r - a.i * b.i, a.r * b.i + a.i * b.r}; }
__device__ __forceinline__ cplx csq(cplx a) { return {a.r * a.r - a.i * a.i, 2.f * a.r * a.i}; }
__device__ __forceinline__ cplx cexp_p(float zr2, float zrev, float p) {
  float e = __builtin_amdgcn_exp2f(zr2 * p);
  float a = __builtin_amdgcn_fractf(zrev * p);
  return {e * __builtin_amdgcn_cosf(a), e * __builtin_amdgcn_sinf(a)};
}

constexpr int NB_KG = 512, NB_LN = 128, NB_T1 = 224, NB_T2 = 224;

__global__ __launch_bounds__(256) void prep_k(const u16* __restrict__ x, const u16* __restrict__ lar, const u16* __restrict__ aim,
    const u16* __restrict__ cre, const u16* __restrict__ cim, const u16* __restrict__ ldt,
    const u16* __restrict__ w1, const u16* __restrict__ w2, char* __restrict__ ws) {
  __shared__ __attribute__((aligned(16))) char smem[24 * 1024];
  const int tid = threadIdx.x, lane = tid & 63, w = tid >> 6;
  int b = blockIdx.x;
  if (b < NB_KG) {
    const int d = b;
    float* cst = (float*)smem;
    f32x4* red = (f32x4*)(smem + 6144);
    u16* kp = (u16*)(smem + 6144 + 16384);
    float* sred = (float*)(smem + 6144 + 16384 + 1312);
    float zr2 = 0, zrev = 0, Cr = 0, Ci = 0;
    if (tid < 128) {
      float dt = rbf(expf(bf2f(ldt[d])));
      float ea = rbf(expf(bf2f(lar[d * NS + tid])));
      float ar = -ea * dt, ai = bf2f(aim[d * NS + tid]) * dt;
      Cr = bf2f(cre[d * NS + tid]) * dt; Ci = bf2f(cim[d * NS + tid]) * dt;
      zr2 = ar * 1.4426950408889634f; zrev = ai * 0.15915494309189535f;
      float s = fabsf(Cr) + fabsf(Ci);
      for (int o = 32; o; o >>= 1) s += __shfl_xor(s, o);
      if (lane == 0) sred[w] = s;
    }
    __syncthreads();
    float B2 = 2.f * (sred[0] + sred[1]);
    int e = 0;
    if (B2 > 0.f && B2 < 3.0e38f) { (void)frexpf(B2, &e); }
    int se = 14 - e; se = se > 120 ? 120 : (se < -120 ? -120 : se);
    float scale = __uint_as_float((unsigned)(se + 127) << 23);
    if (tid < 128) {
      cplx C0 = {Cr * (2.f * scale), Ci * (2.f * scale)};
      cplx C1 = cmul(C0, cexp_p(zr2, zrev, 256.f));
      cplx C2 = cmul(C0, cexp_p(zr2, zrev, 512.f));
      cplx C3 = cmul(C0, cexp_p(zr2, zrev, 1024.f));
      float* c = cst + tid * 12;
      *(f32x4*)c = f32x4{zr2, zrev, C0.r, C0.i};
      *(f32x4*)(c + 4) = f32x4{C1.r, C1.i, C2.r, C2.i};
      c[8] = C3.r; c[9] = C3.i;
    }
    if (tid == 0) ((float*)(ws + WS_KINV))[d] = __uint_as_float((unsigned)(127 - se) << 23);
    if (tid < 32) *(u32x4*)(ws + WS_XRT + ((size_t)d * XR + 2048) * 2 + tid * 16) = u32x4{0, 0, 0, 0};
    __syncthreads();
    const int t = lane & 15, kq = lane >> 4;
    const float tf = (float)t;
    f32x4 acc[4] = {};
#pragma unroll
    for (int ss = 0; ss < 2; ++ss) {
      f16x8 A[4], Bv[4];
#pragma unroll
      for (int mm = 0; mm < 4; ++mm) {
        const float* c = cst + (16 * (2 * w + ss) + 4 * kq + mm) * 12;
        f32x4 c0 = *(const f32x4*)c, c1 = *(const f32x4*)(c + 4);
        cplx C3 = {c[8], c[9]};
        cplx e1 = cexp_p(c0[0], c0[1], tf);
        cplx e2 = csq(e1), e4 = csq(e2), e8 = csq(e4), e16 = csq(e8), e32 = csq(e16), e64 = csq(e32), e128 = csq(e64);
        cplx G0 = cmul(cplx{c0[2], c0[3]}, e16), G1 = cmul(cplx{c1[0], c1[1]}, e32), G2 = cmul(cplx{c1[2], c1[3]}, e64), G3 = cmul(C3, e128);
        A[0][2 * mm] = (_Float16)G0.r; A[0][2 * mm + 1] = (_Float16)(-G0.i);
        A[1][2 * mm] = (_Float16)G1.r; A[1][2 * mm + 1] = (_Float16)(-G1.i);
        A[2][2 * mm] = (_Float16)G2.r; A[2][2 * mm + 1] = (_Float16)(-G2.i);
        A[3][2 * mm] = (_Float16)G3.r; A[3][2 * mm + 1] = (_Float16)(-G3.i);
        Bv[0][2 * mm] = (_Float16)e1.r; Bv[0][2 * mm + 1] = (_Float16)e1.i;
        Bv[1][2 * mm] = (_Float16)e2.r; Bv[1][2 * mm + 1] = (_Float16)e2.i;
        Bv[2][2 * mm] = (_Float16)e4.r; Bv[2][2 * mm + 1] = (_Float16)e4.i;
        Bv[3][2 * mm] = (_Float16)e8.r; Bv[3][2 * mm + 1] = (_Float16)e8.i;
      }
#pragma unroll
      for (int r = 0; r < 4; ++r) acc[r] = __builtin_amdgcn_mfma_f32_16x16x32_f16(A[r], Bv[r], acc[r], 0, 0, 0);
    }
#pragma unroll
    for (int r = 0; r < 4; ++r) red[(w * 4 + r) * 64 + lane] = acc[r];
    __syncthreads();
    {
      const int r = w;
      f32x4 v = red[(0 * 4 + r) * 64 + lane] + red[(1 * 4 + r) * 64 + lane] + red[(2 * 4 + r) * 64 + lane] + red[(3 * 4 + r) * 64 + lane];
      const int base = r == 0 ? 0 : 128 + 128 * r;
#pragma unroll
      for (int j = 0; j < 4; ++j) {
        int hi = 4 * kq + j;
        bool ok = (r == 0) || (hi < 8) || (r == 3 && hi == 8 && t == 0);
        if (ok) kp[base + 16 * hi + t] = (u16)f2h(v[j]);
      }
    }
    __syncthreads();
    {
      unsigned o[4];
#pragma unroll
      for (int j = 0; j < 4; ++j) {
        unsigned v2[2];
#pragma unroll
        for (int q = 0; q < 2; ++q) {
          int l = 8 * tid + 2 * j + q;
          int idx = l < 128 ? l : (int)f2bf((float)l) - 17024;
          v2[q] = kp[idx];
        }
        o[j] = v2[0] | (v2[1] << 16);
      }
      *(u32x4*)(ws + WS_KF + ((size_t)d * L + 8 * tid) * 2) = u32x4{o[0], o[1], o[2], o[3]};
    }
    return;
  }
  b -= NB_KG;
  if (b < NB_LN) {
    const int l0 = b * 16;
    u32x4 v[4];
#pragma unroll
    for (int r = 0; r < 4; ++r) v[r] = *(const u32x4*)(x + (size_t)(l0 + 4 * w + r) * D + lane * 8);
    float f[4][8], s[4], q[4];
#pragma unroll
    for (int r = 0; r < 4; ++r) {
      s[r] = 0;
#pragma unroll
      for (int i = 0; i < 4; ++i) { f[r][2 * i] = bf2f(v[r][i] & 0xffff); f[r][2 * i + 1] = bf2f(v[r][i] >> 16); s[r] += f[r][2 * i] + f[r][2 * i + 1]; }
    }
#pragma unroll
    for (int o = 32; o; o >>= 1)
#pragma unroll
      for (int r = 0; r < 4; ++r) s[r] += __shfl_xor(s[r], o);
#pragma unroll
    for (int r = 0; r < 4; ++r) {
      s[r] *= (1.f / D); q[r] = 0;
#pragma unroll
      for (int i = 0; i < 8; ++i) { float dd = f[r][i] - s[r]; q[r] += dd * dd; }
    }
#pragma unroll
    for (int o = 32; o; o >>= 1)
#pragma unroll
      for (int r = 0; r < 4; ++r) q[r] += __shfl_xor(q[r], o);
    unsigned hv[8][2];
#pragma unroll
    for (int r = 0; r < 4; ++r) {
      float m = rbf(s[r]), vv = rbf(q[r] * (1.f / D));
      float den = rbf(sqrtf(rbf(vv + 1e-5f)));
      unsigned o[4];
#pragma unroll
      for (int i = 0; i < 8; ++i) {
        float y = rbf(rbf(f[r][i] - m) / den);
        f[r][i] = y;
        unsigned hb = f2h(y);
        if (((3 - r) & 1) == 0) hv[i][(3 - r) >> 1] = hb | (((3 - r) & 1) ? 0 : 0);
      }
#pragma unroll
      for (int i = 0; i < 4; ++i) o[i] = pk2(f[r][2 * i], f[r][2 * i + 1]);
      *(u32x4*)(ws + WS_XN + ((size_t)(l0 + 4 * w + r) * D + lane * 8) * 2) = u32x4{o[0], o[1], o[2], o[3]};
    }
#pragma unroll
    for (int i = 0; i < 8; ++i) {
      unsigned lo = f2h(f[3][i]) | (f2h(f[2][i]) << 16);
      unsigned hi = f2h(f[1][i]) | (f2h(f[0][i]) << 16);
      *(u32x2*)(smem + (i * 64 + lane) * 40 + (3 - w) * 8) = u32x2{lo, hi};
    }
    __syncthreads();
#pragma unroll
    for (int k = 0; k < 2; ++k) {
      int dd = tid + 256 * k;
      const char* p = smem + ((dd & 7) * 64 + (dd >> 3)) * 40;
      u32x2 a0 = *(const u32x2*)p, a1 = *(const u32x2*)(p + 8), a2 = *(const u32x2*)(p + 16), a3 = *(const u32x2*)(p + 24);
      char* dst = ws + WS_XRT + ((size_t)dd * XR + (2032 - l0)) * 2;
      *(u32x4*)dst = u32x4{a0[0], a0[1], a1[0], a1[1]};
      *(u32x4*)(dst + 16) = u32x4{a2[0], a2[1], a3[0], a3[1]};
    }
    return;
  }
  b -= NB_LN;
  {
    const u16* src; u16* dst; int R, C, tr, tc;
    if (b < NB_T1) { src = w1; dst = (u16*)(ws + WS_W1T); R = D; C = F; tr = b / 28; tc = b % 28; }
    else { b -= NB_T1; src = w2; dst = (u16*)(ws + WS_W2T); R = F; C = D; tr = b / 8; tc = b % 8; }
    u16* tile = (u16*)smem;
#pragma unroll
    for (int k = 0; k < 2; ++k) {
      int idx = tid + 256 * k; int r = idx >> 3, c8 = idx & 7;
      u32x4 v = *(const u32x4*)(src + (size_t)(tr * 64 + r) * C + tc * 64 + c8 * 8);
      unsigned* p = (unsigned*)(tile + r * 66 + c8 * 8);
      p[0] = v[0]; p[1] = v[1]; p[2] = v[2]; p[3] = v[3];
    }
    __syncthreads();
#pragma unroll
    for (int k = 0; k < 2; ++k) {
      int idx = tid + 256 * k; int c = idx >> 3, r8 = idx & 7;
      unsigned o[4];
#pragma unroll
      for (int i = 0; i < 4; ++i) o[i] = (unsigned)tile[(r8 * 8 + 2 * i) * 66 + c] | ((unsigned)tile[(r8 * 8 + 2 * i + 1) * 66 + c] << 16);
      *(u32x4*)(dst + (size_t)(tc * 64 + c) * R + tr * 64 + r8 * 8) = u32x4{o[0], o[1], o[2], o[3]};
    }
  }
}

__device__ __forceinline__ float gelu_f(float v) {
  float u = 0.7978845608028654f * (v + 0.044715f * v * v * v);
  float e = __builtin_amdgcn_exp2f(-2.f * 1.4426950408889634f * u);
  return v * __builtin_amdgcn_rcpf(1.f + e);
}

__global__ __launch_bounds__(256) void gemm1_k(const u16* __restrict__ b1, char* __restrict__ ws) {
  __shared__ f32x4 xch[2][28][64];
  const int tid = threadIdx.x, lane = tid & 63, w = tid >> 6, r16 = lane & 15, kq = lane >> 4;
  const int bx = blockIdx.x, xcd = bx & 7, idx = bx >> 3;
  const int ft = (xcd & 1) * 8 + (idx & 7), lt = (xcd >> 1) * 4 + (idx >> 3);
  const int lh = w & 1, kh = w >> 1;
  const int f0 = ft * 112, l0 = lt * 128 + lh * 64;
  const u16* Ap = (const u16*)(ws + WS_W1T) + (size_t)(f0 + r16) * D + kh * 256 + kq * 8;
  const u16* Bp = (const u16*)(ws + WS_XN) + (size_t)(l0 + r16) * D + kh * 256 + kq * 8;
  f32x4 acc[7][4] = {};
  constexpr int PD = 3;
  bf16x8 a[PD][7], bb[PD][4];
#pragma unroll
  for (int s = 0; s < PD - 1; ++s) {
#pragma unroll
    for (int i = 0; i < 7; ++i) a[s][i] = *(const bf16x8*)(Ap + i * 16 * D + s * 32);
#pragma unroll
    for (int j = 0; j < 4; ++j) bb[s][j] = *(const bf16x8*)(Bp + j * 16 * D + s * 32);
  }
#pragma unroll
  for (int s = 0; s < 8; ++s) {
    if (s + PD - 1 < 8) {
#pragma unroll
      for (int i = 0; i < 7; ++i) a[(s + PD - 1) % PD][i] = *(const bf16x8*)(Ap + i * 16 * D + (s + PD - 1) * 32);
#pragma unroll
      for (int j = 0; j < 4; ++j) bb[(s + PD - 1) % PD][j] = *(const bf16x8*)(Bp + j * 16 * D + (s + PD - 1) * 32);
    }
#pragma unroll
    for (int i = 0; i < 7; ++i)
#pragma unroll
      for (int j = 0; j < 4; ++j) acc[i][j] = __builtin_amdgcn_mfma_f32_16x16x32_bf16(a[s % PD][i], bb[s % PD][j], acc[i][j], 0, 0, 0);
  }
#pragma unroll
  for (int i = 0; i < 7; ++i)
#pragma unroll
    for (int j = 0; j < 4; ++j)
      if ((i < 4) != (kh == 0)) xch[lh][i * 4 + j][lane] = acc[i][j];
  __syncthreads();
  u16* H = (u16*)(ws + WS_H);
#pragma unroll
  for (int i = 0; i < 7; ++i) {
    if ((i < 4) == (kh == 0)) {
      const int f = f0 + 16 * i + 4 * kq;
      u32x2 bv = *(const u32x2*)(b1 + f);
      float bf[4] = {bf2f(bv[0] & 0xffff), bf2f(bv[0] >> 16), bf2f(bv[1] & 0xffff), bf2f(bv[1] >> 16)};
#pragma unroll
      for (int j = 0; j < 4; ++j) {
        f32x4 v = acc[i][j] + xch[lh][i * 4 + j][lane];
        float g[4];
#pragma unroll
        for (int r = 0; r < 4; ++r) g[r] = gelu_f(rbf(rbf(v[r]) + bf[r]));
        *(u32x2*)(H + (size_t)(l0 + 16 * j + r16) * F + f) = u32x2{pk2(g[0], g[1]), pk2(g[2], g[3])};
      }
    }
  }
}

constexpr int KCS = 2080;
constexpr int CONV_LDS = XR * 2 + 4 * KCS * 2;
__global__ __launch_bounds__(128) void conv_k(const u16* __restrict__ dsk, char* __restrict__ ws) {
  __shared__ __attribute__((aligned(16))) char smem[2 * CONV_LDS];
  const int tid = threadIdx.x, lane = tid & 63, w = tid >> 6, r16 = lane & 15, kq = lane >> 4;
  const int d = blockIdx.x * 2 + w;
  char* sx = smem + w * CONV_LDS;
  char* sk = sx + XR * 2;
  const char* gx = ws + WS_XRT + (size_t)d * XR * 2;
  const char* gk = ws + WS_KF + (size_t)d * L * 2;
#pragma unroll
  for (int it = 0; it < 5; ++it) {
    int c = lane + 64 * it;
    if (c < XR / 8) *(u32x4*)(sx + c * 16) = *(const u32x4*)(gx + c * 16);
  }
#pragma unroll
  for (int it = 0; it < 5; ++it) {
    int g = lane + 64 * it;
    if (g < 258) {
      u32x4 A = {0, 0, 0, 0}, Bq = {0, 0, 0, 0};
      if (g >= 2) A = *(const u32x4*)(gk + (g - 2) * 16);
      if (g >= 1 && g <= 256) Bq = *(const u32x4*)(gk + (g - 1) * 16);
      u32x4 c1 = {__builtin_amdgcn_alignbit(A[1], A[0], 16), __builtin_amdgcn_alignbit(A[2], A[1], 16),
                  __builtin_amdgcn_alignbit(A[3], A[2], 16), __builtin_amdgcn_alignbit(Bq[0], A[3], 16)};
      u32x4 c2 = {A[1], A[2], A[3], Bq[0]};
      u32x4 c3 = {c1[1], c1[2], c1[3], __builtin_amdgcn_alignbit(Bq[1], Bq[0], 16)};
      *(u32x4*)(sk + 0 * KCS * 2 + g * 16) = A;
      *(u32x4*)(sk + 1 * KCS * 2 + g * 16) = c1;
      *(u32x4*)(sk + 2 * KCS * 2 + g * 16) = c2;
      *(u32x4*)(sk + 3 * KCS * 2 + g * 16) = c3;
    }
  }
  __syncthreads();
  f32x4 acc[8] = {};
  const char* pa = sx + (2032 - 16 * r16 + 8 * kq) * 2;
  const int n1 = r16 + 1;
  const char* pb = sk + (n1 & 3) * KCS * 2 + (8 * kq + (n1 & ~3)) * 2;
#pragma unroll 1
  for (int rho = 0; rho < 8; ++rho) {
    f16x8 a[8], bq[8];
#pragma unroll
    for (int tp = 0; tp < 8; ++tp) a[tp] = *(const f16x8*)(pa + (32 * (rho - 8 * tp)) * 2);
#pragma unroll
    for (int sg = 0; sg < 8; ++sg) {
      const char* p = pb + (32 * (8 * sg + rho)) * 2;
      u32x2 lo = *(const u32x2*)p, hi = *(const u32x2*)(p + 8);
      u32x4 v = {lo[0], lo[1], hi[0], hi[1]};
      bq[sg] = __builtin_bit_cast(f16x8, v);
    }
#pragma unroll
    for (int sg = 0; sg < 8; ++sg)
#pragma unroll
      for (int tp = 0; tp < 8 - sg; ++tp)
        acc[sg + tp] = __builtin_amdgcn_mfma_f32_16x16x32_f16(a[tp], bq[sg], acc[sg + tp], 0, 0, 0);
  }
  const float kinv = ((const float*)(ws + WS_KINV))[d];
  const float dk = bf2f(dsk[d]);
  u16* so = (u16*)sk;
  __builtin_amdgcn_s_waitcnt(0);
#pragma unroll
  for (int t = 0; t < 8; ++t)
#pragma unroll
    for (int r = 0; r < 4; ++r) {
      int l = 256 * t + 16 * (4 * kq + r) + r16;
      float xv = h2f(*(const u16*)(sx + (2047 - l) * 2));
      float y = rbf(acc[t][r] * kinv);
      so[l] = (u16)f2bf(y + rbf(xv * dk));
    }
  __builtin_amdgcn_s_waitcnt(0);
  char* go = ws + WS_YAT + (size_t)d * L * 2;
#pragma unroll
  for (int it = 0; it < 4; ++it) {
    int c = lane + 64 * it;
    *(u32x4*)(go + c * 16) = *(const u32x4*)((char*)so + c * 16);
  }
}

__global__ __launch_bounds__(256) void gemm2_k(const u16* __restrict__ x, const u16* __restrict__ b2, char* __restrict__ ws, u16* __restrict__ out) {
  __shared__ f32x4 xch[4][12][64];
  const int tid = threadIdx.x, lane = tid & 63, w = tid >> 6, r16 = lane & 15, kq = lane >> 4;
  const int bx = blockIdx.x, xcd = bx & 7, idx = bx >> 3;
  const int dt_ = idx & 7, lt = xcd * 4 + (idx >> 3);
  const int d0 = dt_ * 64, l0 = lt * 64;
  const u16* Ap = (const u16*)(ws + WS_W2T) + (size_t)(d0 + r16) * F + w * 448 + kq * 8;
  const u16* Bp = (const u16*)(ws + WS_H) + (size_t)(l0 + r16) * F + w * 448 + kq * 8;
  f32x4 acc[4][4] = {};
  constexpr int PD = 4;
  bf16x8 a[PD][4], bb[PD][4];
#pragma unroll
  for (int s = 0; s < PD - 1; ++s) {
#pragma unroll
    for (int i = 0; i < 4; ++i) a[s][i] = *(const bf16x8*)(Ap + (size_t)i * 16 * F + s * 32);
#pragma unroll
    for (int j = 0; j < 4; ++j) bb[s][j] = *(const bf16x8*)(Bp + (size_t)j * 16 * F + s * 32);
  }
#pragma unroll
  for (int s = 0; s < 14; ++s) {
    if (s + PD - 1 < 14) {
#pragma unroll
      for (int i = 0; i < 4; ++i) a[(s + PD - 1) % PD][i] = *(const bf16x8*)(Ap + (size_t)i * 16 * F + (s + PD - 1) * 32);
#pragma unroll
      for (int j = 0; j < 4; ++j) bb[(s + PD - 1) % PD][j] = *(const bf16x8*)(Bp + (size_t)j * 16 * F + (s + PD - 1) * 32);
    }
#pragma unroll
    for (int i = 0; i < 4; ++i)
#pragma unroll
      for (int j = 0; j < 4; ++j) acc[i][j] = __builtin_amdgcn_mfma_f32_16x16x32_bf16(a[s % PD][i], bb[s % PD][j], acc[i][j], 0, 0, 0);
  }
#pragma unroll
  for (int j = 0; j < 4; ++j) {
    if (j != w) {
      int slot = (w > j) ? w - 1 : w;
#pragma unroll
      for (int i = 0; i < 4; ++i) xch[j][slot * 4 + i][lane] = acc[i][j];
    }
  }
  __syncthreads();
  const int l = l0 + 16 * w + r16;
  const u16* Y = (const u16*)(ws + WS_YAT);
#pragma unroll
  for (int j = 0; j < 4; ++j) {
    if (j == w) {
#pragma unroll
      for (int i = 0; i < 4; ++i) {
        f32x4 v = acc[i][j] + xch[j][i][lane] + xch[j][4 + i][lane] + xch[j][8 + i][lane];
        const int dd = d0 + 16 * i + 4 * kq;
        u32x2 bv = *(const u32x2*)(b2 + dd);
        u32x2 xv = *(const u32x2*)(x + (size_t)l * D + dd);
        float bf[4] = {bf2f(bv[0] & 0xffff), bf2f(bv[0] >> 16), bf2f(bv[1] & 0xffff), bf2f(bv[1] >> 16)};
        float xf[4] = {bf2f(xv[0] & 0xffff), bf2f(xv[0] >> 16), bf2f(xv[1] & 0xffff), bf2f(xv[1] >> 16)};
        float o[4];
#pragma unroll
        for (int r = 0; r < 4; ++r) {
          float ya = bf2f(Y[(size_t)(dd + r) * L + l]);
          float m2 = rbf(rbf(v[r]) + bf[r]);
          o[r] = rbf(xf[r] + ya) + m2;
        }
        *(u32x2*)(out + (size_t)l * D + dd) = u32x2{pk2(o[0], o[1]), pk2(o[2], o[3])};
      }
    }
  }
}

extern "C" void kernel_launch(void* const* d_in, const int* in_sizes, int n_in,
                              void* d_out, int out_size, void* d_ws, size_t ws_size,
                              hipStream_t stream) {
  const u16* x = (const u16*)d_in[0]; const u16* lar = (const u16*)d_in[1]; const u16* aim = (const u16*)d_in[2];
  const u16* cr = (const u16*)d_in[3]; const u16* ci = (const u16*)d_in[4]; const u16* ldt = (const u16*)d_in[5];
  const u16* dsk = (const u16*)d_in[6]; const u16* w1 = (const u16*)d_in[8]; const u16* b1 = (const u16*)d_in[9];
  const u16* w2 = (const u16*)d_in[10]; const u16* b2 = (const u16*)d_in[11];
  char* ws = (char*)d_ws;
  prep_k<<<NB_KG + NB_LN + NB_T1 + NB_T2, 256, 0, stream>>>(x, lar, aim, cr, ci, ldt, w1, w2, ws);
  gemm1_k<<<256, 256, 0, stream>>>(b1, ws);
  conv_k<<<256, 128, 0, stream>>>(dsk, ws);
  gemm2_k<<<256, 256, 0, stream>>>(x, b2, ws, (u16*)d_out);
}
```

```cpp
#include <hip/hip_runtime.h>
#include <stdint.h>

typedef unsigned short u16;
typedef __attribute__((ext_vector_type(8))) __bf16 bf16x8;
typedef __attribute__((ext_vector_type(8))) _Float16 f16x8;
typedef __attribute__((ext_vector_type(4))) short s16x4;
typedef __attribute__((ext_vector_type(4))) float f32x4;
typedef __attribute__((ext_vector_type(4))) unsigned u32x4;
typedef __attribute__((ext_vector_type(2))) unsigned u32x2;
typedef __attribute__((address_space(3))) s16x4* lds_s16x4p;

constexpr int L = 2048, D = 512, NS = 128, F = 1792;
constexpr int XR = 2304;

__device__ __forceinline__ float bf2f(unsigned v) { return __uint_as_float(v << 16); }
__device__ __forceinline__ unsigned f2bf(float f) { __bf16 b = (__bf16)f; return (unsigned)__builtin_bit_cast(u16, b); }
__device__ __forceinline__ float rbf(float f) { return bf2f(f2bf(f)); }
__device__ __forceinline__ unsigned pk2(float a, float b) { return f2bf(a) | (f2bf(b) << 16); }
__device__ __forceinline__ unsigned f2h(float f) { _Float16 h = (_Float16)f; return (unsigned)__builtin_bit_cast(u16, h); }
__device__ __forceinline__ float h2f(unsigned v) { return (float)__builtin_bit_cast(_Float16, (u16)v); }

constexpr size_t WS_YFLAG = 0;
constexpr size_t WS_KINV = 4096;
constexpr size_t WS_XRT = 8192;
constexpr size_t WS_W2T = WS_XRT + (size_t)D * XR * 2;
constexpr size_t WS_KF  = WS_W2T + (size_t)F * D * 2;
constexpr size_t WS_HT  = WS_KF + (size_t)D * L * 2;
constexpr size_t WS_YAT = WS_HT + (size_t)L * F * 2;

#define STAMP(kid,i)
typedef __attribute__((ext_vector_type(2))) float f32x2;
__device__ __forceinline__ f32x2 cmul(f32x2 a, f32x2 b) {
  f32x2 t, o;
  asm("v_pk_mul_f32 %0, %1, %2 op_sel:[0,0] op_sel_hi:[0,1]" : "=v"(t) : "v"(a), "v"(b));
  asm("v_pk_fma_f32 %0, %1, %2, %3 op_sel:[1,1,0] op_sel_hi:[1,0,1] neg_lo:[1,0,0]" : "=v"(o) : "v"(a), "v"(b), "v"(t));
  return o;
}
__device__ __forceinline__ f32x2 csq(f32x2 a) { return cmul(a, a); }
__device__ __forceinline__ f32x2 cexp_p(float zr2, float zrev, float p) {
  float e = __builtin_amdgcn_exp2f(zr2 * p);
  float a = __builtin_amdgcn_fractf(zrev * p);
  f32x2 cs = {__builtin_amdgcn_cosf(a), __builtin_amdgcn_sinf(a)};
  return cs * e;
}
__device__ __forceinline__ unsigned pkh(float a, float b) {
  typedef __attribute__((ext_vector_type(2))) _Float16 f16x2;
  f16x2 h = {(_Float16)a, (_Float16)b};
  return __builtin_bit_cast(unsigned, h);
}
__device__ __forceinline__ unsigned pkbf(f32x2 v) {
  typedef __attribute__((ext_vector_type(2))) __bf16 bf16x2;
  bf16x2 h = {(__bf16)v[0], (__bf16)v[1]};
  return __builtin_bit_cast(unsigned, h);
}
__device__ __forceinline__ f32x2 upbf(unsigned v) { return f32x2{__uint_as_float(v << 16), __uint_as_float(v & 0xffff0000u)}; }
__device__ __forceinline__ float gelu_f(float v) {
  float u = 0.7978845608028654f * (v + 0.044715f * v * v * v);
  float e = __builtin_amdgcn_exp2f(-2.f * 1.4426950408889634f * u);
  return v * __builtin_amdgcn_rcpf(1.f + e);
}

__device__ __forceinline__ void glds16(const void* gsrc, unsigned lds_dst) {
  unsigned keep;
  asm volatile("s_mov_b32 %0, m0\n\ts_mov_b32 m0, %2\n\ts_nop 0\n\tglobal_load_lds_dwordx4 %1, off\n\ts_mov_b32 m0, %0"
               : "=&s"(keep) : "v"(gsrc), "s"(lds_dst) : "memory");
}
#define WAIT_VM(n) asm volatile("s_waitcnt vmcnt(" #n ")" ::: "memory")
#define WAIT_LGKM0 asm volatile("s_waitcnt lgkmcnt(0)" ::: "memory")

__device__ __forceinline__ void lds_arrive(unsigned* cnt, int lane) {
  WAIT_LGKM0;
  if (lane == 0) __hip_atomic_fetch_add(cnt, 1u, __ATOMIC_RELAXED, __HIP_MEMORY_SCOPE_WORKGROUP);
}
__device__ __forceinline__ void lds_wait(unsigned* cnt, unsigned target) {
  while (__hip_atomic_load(cnt, __ATOMIC_RELAXED, __HIP_MEMORY_SCOPE_WORKGROUP) < target) __builtin_amdgcn_s_sleep(1);
  asm volatile("" ::: "memory");
}
__device__ __forceinline__ bf16x8 tr_frag(const char* p) {
  s16x4 lo = __builtin_amdgcn_ds_read_tr16_b64_v4i16((lds_s16x4p)(p));
  s16x4 hi = __builtin_amdgcn_ds_read_tr16_b64_v4i16((lds_s16x4p)(p + 512));
  typedef __attribute__((ext_vector_type(8))) short s16x8;
  s16x8 v = {lo[0], lo[1], lo[2], lo[3], hi[0], hi[1], hi[2], hi[3]};
  return __builtin_bit_cast(bf16x8, v);
}

constexpr int RING = 4;
constexpr int A_XT = 0;
constexpr int A_RING = 65536;
constexpr int A_AUX = A_RING + 4 * RING * 4096;
constexpr int AUX_CST = 0;
constexpr int AUX_PART = 6144;
constexpr int AUX_KP = 10240;
constexpr int AUX_W2 = 11776;
constexpr int AUX_SZ = 15872;
constexpr int A_FLAG = A_AUX + 2 * AUX_SZ;
constexpr int A_LDS = A_FLAG + 64;
static_assert(A_LDS <= 163840, "lds");

__device__ __forceinline__ void ln_load(const u16* __restrict__ x, int l0, int w, int lane, u32x4 (&v)[8]) {
  const int rr = lane >> 3, c = lane & 7;
#pragma unroll
  for (int i = 0; i < 8; ++i) v[i] = *(const u32x4*)(x + (size_t)(l0 + 8 * w + rr) * D + 64 * i + 8 * c);
}
__device__ __forceinline__ void ln_tile(const u32x4 (&v)[8], int w, int lane, char* smem) {
  const int rr = lane >> 3, c = lane & 7;
  f32x2 e[32];
  f32x2 s2 = {0.f, 0.f};
#pragma unroll
  for (int i = 0; i < 8; ++i)
#pragma unroll
    for (int j = 0; j < 4; ++j) { e[4 * i + j] = upbf(v[i][j]); s2 += e[4 * i + j]; }
  float s = s2[0] + s2[1];
  s += __shfl_xor(s, 1); s += __shfl_xor(s, 2); s += __shfl_xor(s, 4);
  const float mean = s * (1.f / D);
  f32x2 q2 = {0.f, 0.f};
#pragma unroll
  for (int i = 0; i < 32; ++i) { f32x2 t = e[i] - mean; q2 = __builtin_elementwise_fma(t, t, q2); }
  float q = q2[0] + q2[1];
  q += __shfl_xor(q, 1); q += __shfl_xor(q, 2); q += __shfl_xor(q, 4);
  const float m = rbf(mean), vv = rbf(q * (1.f / D));
  const float den = rbf(sqrtf(rbf(vv + 1e-5f)));
  const float rc = 1.0f / den;
  const f32x2 rc2 = {rc, rc}, nden2 = {-den, -den};
  const int j16 = w >> 1, r = 8 * (w & 1) + rr;
#pragma unroll
  for (int i = 0; i < 8; ++i) {
    u32x4 o;
#pragma unroll
    for (int j = 0; j < 4; ++j) {
      f32x2 a = upbf(pkbf(e[4 * i + j] - m));
      f32x2 qq = a * rc2;
      f32x2 rem = __builtin_elementwise_fma(qq, nden2, a);
      qq = __builtin_elementwise_fma(rem, rc2, qq);
      o[j] = pkbf(qq);
    }
    const int ks = 2 * i + (c >> 2), kq = c & 3;
    *(u32x4*)(smem + A_XT + ((((j16 * 16 + ks) * 4 + kq) * 16) + (r ^ (kq | ((ks & 1) << 2)))) * 16) = o;
  }
}

__global__ __launch_bounds__(512, 2) void kA(const u16* __restrict__ x, const u16* __restrict__ lar, const u16* __restrict__ aim,
    const u16* __restrict__ cre, const u16* __restrict__ cim, const u16* __restrict__ ldt,
    const u16* __restrict__ w1, const u16* __restrict__ b1, const u16* __restrict__ w2, char* __restrict__ ws) {
  __shared__ __attribute__((aligned(16))) char smem[A_LDS];
  const int tid = threadIdx.x, lane = tid & 63, r16 = lane & 15, kq = lane >> 4;
  const int w = __builtin_amdgcn_readfirstlane(tid >> 6);
  const int b = blockIdx.x;
  unsigned* flags = (unsigned*)(smem + A_FLAG);
  const unsigned sbase = (unsigned)(size_t)smem;
  STAMP(0,0);
  if (tid < 16) flags[tid] = 0;
  __syncthreads();
  const bool isg = b < 224;
  const int lt = isg ? (b & 7) * 4 + ((b >> 3) & 3) : b - 224, ft = (b >> 3) >> 2;
  const int l0 = lt * 64;
  const int drow = lane >> 3, dsl = lane & 7;
  const int q4 = (lane & 15) >> 2, p4 = lane & 3;
  const int frd = ((q4 >> 1) & 1) | ((kq & 1) << 1);
  u32x4 xv[8];
  if (w < 4) {
    const int fw0 = ft * 256 + w * 64;
    const unsigned ring = sbase + A_RING + w * (RING * 4096);
    const u16* wsrc[2];
#pragma unroll
    for (int qq = 0; qq < 2; ++qq) {
      int fr = ((drow >> 1) & 1) | (qq << 1);
      wsrc[qq] = w1 + (size_t)drow * F + fw0 + (((dsl >> 1) ^ fr) * 16 + (dsl & 1) * 8);
    }
    auto issue = [&](int s) {
#pragma unroll
      for (int q = 0; q < 4; ++q) glds16(wsrc[q & 1] + (size_t)(32 * s + 8 * q) * F, ring + (s % RING) * 4096 + q * 1024);
    };
    ln_load(x, l0, w, lane, xv);
    if (isg) {
#pragma unroll
      for (int s = 0; s < RING - 1; ++s) issue(s);
    }
    ln_tile(xv, w, lane, smem);
    STAMP(0,1);
    lds_arrive(&flags[0], lane);
    lds_wait(&flags[0], 8);
    STAMP(0,2);
    if (isg) {
      const char* ap[4];
#pragma unroll
      for (int i = 0; i < 4; ++i) ap[i] = smem + A_RING + w * (RING * 4096) + (8 * kq + q4) * 128 + ((i ^ frd) * 32) + p4 * 8;
      const char* bp0 = smem + A_XT + (kq * 16 + (r16 ^ kq)) * 16;
      const char* bp1 = smem + A_XT + (kq * 16 + (r16 ^ (kq | 4))) * 16;
      f32x4 acc[4][4] = {};
      bf16x8 a[2][4], bb[2][4];
      auto rdfr = [&](int s, bf16x8 (&aa)[4], bf16x8 (&bq)[4]) {
#pragma unroll
        for (int i = 0; i < 4; ++i) aa[i] = tr_frag(ap[i] + (s % RING) * 4096);
#pragma unroll
        for (int j = 0; j < 4; ++j) bq[j] = *(const bf16x8*)(((s & 1) ? bp1 : bp0) + (j * 16 + s) * 1024);
      };
      WAIT_VM(8);
      rdfr(0, a[0], bb[0]);
#pragma unroll
      for (int s = 0; s < 16; ++s) {
        if (s + RING - 1 < 16) { issue(s + RING - 1); WAIT_VM(8); }
        else if (s + RING - 1 == 16) { WAIT_VM(4); }
        else { WAIT_VM(0); }
        if (s + 1 < 16) rdfr(s + 1, a[(s + 1) & 1], bb[(s + 1) & 1]);
#pragma unroll
        for (int i = 0; i < 4; ++i)
#pragma unroll
          for (int j = 0; j < 4; ++j) acc[i][j] = __builtin_amdgcn_mfma_f32_16x16x32_bf16(a[s & 1][i], bb[s & 1][j], acc[i][j], 0, 0, 0);
      }
      STAMP(0,3);
      char* HT = ws + WS_HT;
#pragma unroll
      for (int i = 0; i < 4; ++i) {
        const int f = fw0 + 16 * i + 4 * kq;
        const u32x2 bv = *(const u32x2*)(b1 + f);
        const f32x2 b01 = upbf(bv[0]), b23 = upbf(bv[1]);
        const int ks = f >> 5, kq2 = (f >> 3) & 3;
#pragma unroll
        for (int j = 0; j < 4; ++j) {
          unsigned o[2];
#pragma unroll
          for (int hh = 0; hh < 2; ++hh) {
            f32x2 v = {acc[i][j][2 * hh], acc[i][j][2 * hh + 1]};
            v = upbf(pkbf(v)) + (hh ? b23 : b01);
            v = upbf(pkbf(v));
            f32x2 t = v * v;
            t = __builtin_elementwise_fma(t, f32x2{-0.1029432397f, -0.1029432397f}, f32x2{-2.302208198f, -2.302208198f});
            t = t * v;
            f32x2 ex = {__builtin_amdgcn_exp2f(t[0]), __builtin_amdgcn_exp2f(t[1])};
            ex = ex + 1.0f;
            f32x2 rc = {__builtin_amdgcn_rcpf(ex[0]), __builtin_amdgcn_rcpf(ex[1])};
            o[hh] = pkbf(v * rc);
          }
          *(u32x2*)(HT + ((((size_t)((l0 >> 4) + j) * 56 + ks) * 64 + kq2 * 16 + r16) * 16) + (f & 7) * 2) = u32x2{o[0], o[1]};
        }
      }
      STAMP(0,4);
    } else {
#pragma unroll 2
      for (int it = 0; it < 16; ++it) {
        const int task = it * 256 + tid;
        const int d = task >> 3, m = task & 7;
        const int ks = d >> 5, kqd = (d >> 3) & 3, hx = kqd | ((ks & 1) << 2);
        unsigned o[4];
#pragma unroll
        for (int e = 0; e < 4; ++e) {
          float v2[2];
#pragma unroll
          for (int h = 0; h < 2; ++h) {
            const int rl = 63 - 8 * m - (2 * e + h);
            const u16 bv = *(const u16*)(smem + A_XT + (((((rl >> 4) * 16 + ks) * 4 + kqd) * 16) + ((rl & 15) ^ hx)) * 16 + (d & 7) * 2);
            v2[h] = bf2f(bv);
          }
          o[e] = pkh(v2[0], v2[1]);
        }
        *(u32x4*)(ws + WS_XRT + ((size_t)d * XR + (1984 - l0) + 8 * m) * 2) = u32x4{o[0], o[1], o[2], o[3]};
      }
      STAMP(0,4);
    }
  } else {
    const int pr = (w - 4) >> 1, role = (w - 4) & 1;
    const int d = 2 * b + pr;
    char* aux = smem + A_AUX + pr * AUX_SZ;
    float* cst = (float*)(aux + AUX_CST + role * 3072);
    f32x4* part = (f32x4*)(aux + AUX_PART);
    u16* kp = (u16*)(aux + AUX_KP);
    const int w2id = d;
    const bool dow2 = (role == 1) && (w2id < 448);
    ln_load(x, l0, w, lane, xv);
    const int n = role * 64 + lane, n2 = (role ^ 1) * 64 + lane;
    u32x4 warm[14];
    {
      const char* wp = (const char*)w1 + ((size_t)((b >> 3) * 4 + (w - 4)) * 14) * 1024 + lane * 16;
#pragma unroll
      for (int i = 0; i < 14; ++i) warm[i] = *(const u32x4*)(wp + i * 1024);
    }
    const unsigned p_ldt = ldt[d], p_lar = lar[d * NS + n], p_aim = aim[d * NS + n], p_cr = cre[d * NS + n], p_ci = cim[d * NS + n],
                   p_cr2 = cre[d * NS + n2], p_ci2 = cim[d * NS + n2];
    if (dow2) {
      const int ks = w2id >> 3, dq = w2id & 7;
#pragma unroll
      for (int q = 0; q < 4; ++q) {
        int fr = ((drow >> 1) & 1) | ((q & 1) << 1);
        glds16(w2 + (size_t)(32 * ks + 8 * q + drow) * D + dq * 64 + (((dsl >> 1) ^ fr) * 16 + (dsl & 1) * 8),
               sbase + A_AUX + pr * AUX_SZ + AUX_W2 + q * 1024);
      }
    }
    ln_tile(xv, w, lane, smem);
    lds_arrive(&flags[0], lane);
#pragma unroll
    for (int i = 0; i < 14; ++i) asm volatile("" :: "v"(warm[i]));
    __builtin_amdgcn_s_setprio(2);
    const float dt = rbf(expf(bf2f(p_ldt)));
    const float ea = rbf(expf(bf2f(p_lar)));
    const float ar = -ea * dt, ai = bf2f(p_aim) * dt;
    const float Cr = bf2f(p_cr) * dt, Ci = bf2f(p_ci) * dt;
    float ssum = fabsf(Cr) + fabsf(Ci) + fabsf(bf2f(p_cr2) * dt) + fabsf(bf2f(p_ci2) * dt);
#pragma unroll
    for (int o = 32; o; o >>= 1) ssum += __shfl_xor(ssum, o);
    const float B2 = 2.f * ssum;
    int e = 0;
    if (B2 > 0.f && B2 < 3.0e38f) { (void)frexpf(B2, &e); }
    int se = 14 - e; se = se > 120 ? 120 : (se < -120 ? -120 : se);
    const float scale = __uint_as_float((unsigned)(se + 127) << 23);
    {
      const float zr2 = ar * 1.4426950408889634f, zrev = ai * 0.15915494309189535f;
      f32x2 C0 = {Cr * (2.f * scale), Ci * (2.f * scale)};
      f32x2 C1 = cmul(C0, cexp_p(zr2, zrev, 256.f));
      f32x2 C2 = cmul(C0, cexp_p(zr2, zrev, 512.f));
      f32x2 C3 = cmul(C0, cexp_p(zr2, zrev, 1024.f));
      float* c = cst + lane * 12;
      *(f32x4*)c = f32x4{zr2, zrev, C0[0], C0[1]};
      *(f32x4*)(c + 4) = f32x4{C1[0], C1[1], C2[0], C2[1]};
      *(f32x4*)(c + 8) = f32x4{C3[0], C3[1], 0.f, 0.f};
    }
    STAMP(0,1);
    const float tf = (float)r16;
    f32x4 acc[4] = {};
#pragma unroll 1
    for (int ss = 0; ss < 4; ++ss) {
      u32x4 A[4], Bv[4];
#pragma unroll
      for (int mm = 0; mm < 4; ++mm) {
        const float* c = cst + (16 * ss + 4 * kq + mm) * 12;
        f32x4 c0 = *(const f32x4*)c, c1 = *(const f32x4*)(c + 4);
        f32x2 c2 = *(const f32x2*)(c + 8);
        f32x2 e1 = cexp_p(c0[0], c0[1], tf);
        f32x2 e2 = csq(e1), e4 = csq(e2), e8 = csq(e4), e16 = csq(e8), e32 = csq(e16), e64 = csq(e32), e128 = csq(e64);
        f32x2 G0 = cmul(f32x2{c0[2], c0[3]}, e16), G1 = cmul(f32x2{c1[0], c1[1]}, e32), G2 = cmul(f32x2{c1[2], c1[3]}, e64), G3 = cmul(c2, e128);
        A[0][mm] = pkh(G0[0], -G0[1]); A[1][mm] = pkh(G1[0], -G1[1]); A[2][mm] = pkh(G2[0], -G2[1]); A[3][mm] = pkh(G3[0], -G3[1]);
        Bv[0][mm] = pkh(e1[0], e1[1]); Bv[1][mm] = pkh(e2[0], e2[1]); Bv[2][mm] = pkh(e4[0], e4[1]); Bv[3][mm] = pkh(e8[0], e8[1]);
      }
#pragma unroll
      for (int r = 0; r < 4; ++r)
        acc[r] = __builtin_amdgcn_mfma_f32_16x16x32_f16(__builtin_bit_cast(f16x8, A[r]), __builtin_bit_cast(f16x8, Bv[r]), acc[r], 0, 0, 0);
    }
    STAMP(0,2);
    if (role == 1) {
#pragma unroll
      for (int r = 0; r < 4; ++r) part[r * 64 + lane] = acc[r];
      lds_arrive(&flags[1 + pr], lane);
      if (dow2) {
        WAIT_VM(0);
        const int ks = w2id >> 3, dq = w2id & 7;
#pragma unroll
        for (int i = 0; i < 4; ++i) {
          bf16x8 v = tr_frag(aux + AUX_W2 + (8 * kq + q4) * 128 + ((i ^ frd) * 32) + p4 * 8);
          *(bf16x8*)(ws + WS_W2T + (((size_t)(dq * 4 + i) * 56 + ks) * 64 + lane) * 16) = v;
        }
      }
    } else {
      lds_wait(&flags[1 + pr], 1);
#pragma unroll
      for (int r = 0; r < 4; ++r) {
        f32x4 v = acc[r] + part[r * 64 + lane];
        const int base = r == 0 ? 0 : 128 + 128 * r;
#pragma unroll
        for (int j = 0; j < 4; ++j) {
          int hi = 4 * kq + j;
          bool ok = (r == 0) || (hi < 8) || (r == 3 && hi == 8 && r16 == 0);
          if (ok) kp[base + 16 * hi + r16] = (u16)f2h(v[j]);
        }
      }
      WAIT_LGKM0;
#pragma unroll
      for (int it = 0; it < 4; ++it) {
        unsigned o[4];
#pragma unroll
        for (int j = 0; j < 4; ++j) {
          unsigned v2[2];
#pragma unroll
          for (int q = 0; q < 2; ++q) {
            int l = 8 * (it * 64 + lane) + 2 * j + q;
            int idx = l < 128 ? l : (int)f2bf((float)l) - 17024;
            v2[q] = kp[idx];
          }
          o[j] = v2[0] | (v2[1] << 16);
        }
        *(u32x4*)(ws + WS_KF + ((size_t)d * L + 8 * (it * 64 + lane)) * 2) = u32x4{o[0], o[1], o[2], o[3]};
      }
      if (lane == 0) {
        ((float*)(ws + WS_KINV))[d] = __uint_as_float((unsigned)(127 - se) << 23);
        *(u32x2*)(ws + WS_YFLAG + d * 8) = u32x2{0, 0};
      }
      if (lane < 32) *(u32x4*)(ws + WS_XRT + ((size_t)d * XR + 2048) * 2 + lane * 16) = u32x4{0, 0, 0, 0};
    }
    STAMP(0,4);
  }
}

constexpr int KCS = 2080;
constexpr int CONV_SZ = XR * 2 + 4 * KCS * 2;
constexpr int B_XCH = 0;
constexpr int B_CONV = 49152;
constexpr int B_CX = B_CONV + 2 * CONV_SZ;
constexpr int B_FLAG = B_CX + 16384;
constexpr int B_LDS = B_FLAG + 64;

__global__ __launch_bounds__(512, 2) void kB(const u16* __restrict__ x, const u16* __restrict__ dsk, const u16* __restrict__ b2,
                                             char* __restrict__ ws, u16* __restrict__ out) {
  __shared__ __attribute__((aligned(16))) char smem[B_LDS];
  const int tid = threadIdx.x, lane = tid & 63, r16 = lane & 15, kq = lane >> 4;
  const int w = __builtin_amdgcn_readfirstlane(tid >> 6);
  const int b = blockIdx.x;
  unsigned* flags = (unsigned*)(smem + B_FLAG);
  STAMP(1,0);
  if (tid < 16) flags[tid] = 0;
  __syncthreads();
  auto yrs = __builtin_amdgcn_make_buffer_rsrc((void*)(ws + WS_YAT), 0, D * L * 2, 0x00020000);
  if (w < 4) {
    const int xcd = b & 7, idx = b >> 3;
    const int dt_ = idx & 7, lt = xcd * 4 + (idx >> 3);
    const int d0 = dt_ * 64, l0 = lt * 64;
    const char* Ap = ws + WS_W2T + (((size_t)(dt_ * 4) * 56 + w * 14) * 64 + lane) * 16;
    const char* Bp = ws + WS_HT + (((size_t)(lt * 4) * 56 + w * 14) * 64 + lane) * 16;
    f32x4 acc[4][4] = {};
    constexpr int PD = 4;
    bf16x8 a[PD][4], bb[PD][4];
#pragma unroll
    for (int s = 0; s < PD - 1; ++s) {
#pragma unroll
      for (int i = 0; i < 4; ++i) a[s][i] = *(const bf16x8*)(Ap + i * (56 * 1024) + s * 1024);
#pragma unroll
      for (int j = 0; j < 4; ++j) bb[s][j] = *(const bf16x8*)(Bp + j * (56 * 1024) + s * 1024);
    }
#pragma unroll
    for (int s = 0; s < 14; ++s) {
      if (s + PD - 1 < 14) {
#pragma unroll
        for (int i = 0; i < 4; ++i) a[(s + PD - 1) % PD][i] = *(const bf16x8*)(Ap + i * (56 * 1024) + (s + PD - 1) * 1024);
#pragma unroll
        for (int j = 0; j < 4; ++j) bb[(s + PD - 1) % PD][j] = *(const bf16x8*)(Bp + j * (56 * 1024) + (s + PD - 1) * 1024);
      }
      __builtin_amdgcn_sched_barrier(0);
#pragma unroll
      for (int i = 0; i < 4; ++i)
#pragma unroll
        for (int j = 0; j < 4; ++j) acc[i][j] = __builtin_amdgcn_mfma_f32_16x16x32_bf16(a[s % PD][i], bb[s % PD][j], acc[i][j], 0, 0, 0);
      __builtin_amdgcn_sched_barrier(0);
    }
    STAMP(1,1);
    f32x4* xch = (f32x4*)(smem + B_XCH);
#pragma unroll
    for (int j = 0; j < 4; ++j) {
      if (j != w) {
        const int slot = (w > j) ? w - 1 : w;
#pragma unroll
        for (int i = 0; i < 4; ++i) xch[(j * 12 + slot * 4 + i) * 64 + lane] = acc[i][j];
      }
    }
    lds_arrive(&flags[0], lane);
    if (w == 0) {
      const unsigned long long* fp = (const unsigned long long*)(ws + WS_YFLAG) + d0 + lane;
      for (int spin = 0; spin < (1 << 22); ++spin) {
        unsigned long long v = __hip_atomic_load(fp, __ATOMIC_RELAXED, __HIP_MEMORY_SCOPE_AGENT);
        if (__all(v == 0x100000001ull)) break;
        __builtin_amdgcn_s_sleep(8);
      }
      asm volatile("" ::: "memory");
      lds_arrive(&flags[3], lane);
    } else {
      lds_wait(&flags[3], 1);
    }
    STAMP(1,2);
    const int l = l0 + 16 * w + r16;
    unsigned yv[4][4];
#pragma unroll
    for (int i = 0; i < 4; ++i)
#pragma unroll
      for (int r = 0; r < 4; ++r)
        yv[i][r] = __builtin_amdgcn_raw_buffer_load_b16(yrs, ((d0 + 16 * i + 4 * kq + r) * L + l) * 2, 0, 16);
    lds_wait(&flags[0], 4);
#pragma unroll
    for (int j = 0; j < 4; ++j) {
      if (j == w) {
#pragma unroll
        for (int i = 0; i < 4; ++i) {
          f32x4 v = acc[i][j] + xch[(j * 12 + i) * 64 + lane] + xch[(j * 12 + 4 + i) * 64 + lane] + xch[(j * 12 + 8 + i) * 64 + lane];
          const int dd = d0 + 16 * i + 4 * kq;
          u32x2 bv = *(const u32x2*)(b2 + dd);
          u32x2 xv = *(const u32x2*)(x + (size_t)l * D + dd);
          float bf[4] = {bf2f(bv[0] & 0xffff), bf2f(bv[0] >> 16), bf2f(bv[1] & 0xffff), bf2f(bv[1] >> 16)};
          float xf[4] = {bf2f(xv[0] & 0xffff), bf2f(xv[0] >> 16), bf2f(xv[1] & 0xffff), bf2f(xv[1] >> 16)};
          float o[4];
#pragma unroll
          for (int r = 0; r < 4; ++r) {
            float ya = bf2f(yv[i][r] & 0xffff);
            float m2 = rbf(rbf(v[r]) + bf[r]);
            o[r] = rbf(xf[r] + ya) + m2;
          }
          *(u32x2*)(out + (size_t)l * D + dd) = u32x2{pk2(o[0], o[1]), pk2(o[2], o[3])};
        }
      }
    }
    STAMP(1,4);
  } else {
    const int pr = (w - 4) >> 1, role = (w - 4) & 1;
    const int d = 2 * b + pr;
    char* sx = smem + B_CONV + pr * CONV_SZ;
    char* sk = sx + XR * 2;
    f32x4* cx = (f32x4*)(smem + B_CX + pr * 8192);
    const char* gx = ws + WS_XRT + (size_t)d * XR * 2;
    const char* gk = ws + WS_KF + (size_t)d * L * 2;
    const float kinv = ((const float*)(ws + WS_KINV))[d];
    const float dk = bf2f(dsk[d]);
    {
      u32x4 xa[3], ka[3], kb[3];
#pragma unroll
      for (int it = 0; it < 3; ++it) {
        const int c = 96 * role + lane + 64 * it;
        xa[it] = *(const u32x4*)(gx + c * 16);
        const int g = 66 * role + lane + 64 * it;
        const int ga = g - 2 < 0 ? 0 : g - 2, gb = g - 1 < 0 ? 0 : (g - 1 > 255 ? 255 : g - 1);
        ka[it] = *(const u32x4*)(gk + ga * 16);
        kb[it] = *(const u32x4*)(gk + gb * 16);
      }
#pragma unroll
      for (int it = 0; it < 3; ++it) {
        const int c = 96 * role + lane + 64 * it;
        *(u32x4*)(sx + c * 16) = xa[it];
        const int g = 66 * role + lane + 64 * it;
        const u32x4 z = {0, 0, 0, 0};
        u32x4 A = g >= 2 ? ka[it] : z, Bq = (g >= 1 && g <= 256) ? kb[it] : z;
        u32x4 c1 = {__builtin_amdgcn_alignbit(A[1], A[0], 16), __builtin_amdgcn_alignbit(A[2], A[1], 16),
                    __builtin_amdgcn_alignbit(A[3], A[2], 16), __builtin_amdgcn_alignbit(Bq[0], A[3], 16)};
        u32x4 c2 = {A[1], A[2], A[3], Bq[0]};
        u32x4 c3 = {c1[1], c1[2], c1[3], __builtin_amdgcn_alignbit(Bq[1], Bq[0], 16)};
        *(u32x4*)(sk + 0 * KCS * 2 + g * 16) = A;
        *(u32x4*)(sk + 1 * KCS * 2 + g * 16) = c1;
        *(u32x4*)(sk + 2 * KCS * 2 + g * 16) = c2;
        *(u32x4*)(sk + 3 * KCS * 2 + g * 16) = c3;
      }
    }
    lds_arrive(&flags[1 + pr], lane);
    lds_wait(&flags[1 + pr], 2);
    STAMP(1,1);
    f32x4 acc[8] = {};
    const char* pa = sx + (2032 - 16 * r16 + 8 * kq + 128 * role) * 2;
    const int n1 = r16 + 1;
    const char* pb = sk + (n1 & 3) * KCS * 2 + (8 * kq + (n1 & ~3) + 128 * role) * 2;
#pragma unroll 1
    for (int rho = 0; rho < 4; ++rho) {
      f16x8 a[8], bq[8];
#pragma unroll
      for (int tp = 0; tp < 8; ++tp) a[tp] = *(const f16x8*)(pa + (32 * (rho - 8 * tp)) * 2);
#pragma unroll
      for (int sg = 0; sg < 8; ++sg) {
        const char* p = pb + (32 * (8 * sg + rho)) * 2;
        u32x2 lo = *(const u32x2*)p, hi = *(const u32x2*)(p + 8);
        u32x4 v = {lo[0], lo[1], hi[0], hi[1]};
        bq[sg] = __builtin_bit_cast(f16x8, v);
      }
#pragma unroll
      for (int sg = 0; sg < 8; ++sg)
#pragma unroll
        for (int tp = 0; tp < 8 - sg; ++tp)
          acc[sg + tp] = __builtin_amdgcn_mfma_f32_16x16x32_f16(a[tp], bq[sg], acc[sg + tp], 0, 0, 0);
    }
    STAMP(1,2);
    f32x4 keep[4];
    if (role) {
#pragma unroll
      for (int t = 0; t < 4; ++t) { cx[(4 + t) * 64 + lane] = acc[t]; keep[t] = acc[4 + t]; }
    } else {
#pragma unroll
      for (int t = 0; t < 4; ++t) { cx[t * 64 + lane] = acc[4 + t]; keep[t] = acc[t]; }
    }
    lds_arrive(&flags[1 + pr], lane);
    lds_wait(&flags[1 + pr], 4);
    u16* so = (u16*)(sk + role * 4096);
#pragma unroll
    for (int t = 0; t < 4; ++t) {
      f32x4 v = keep[t] + cx[((role ^ 1) * 4 + t) * 64 + lane];
#pragma unroll
      for (int r = 0; r < 4; ++r) {
        const int ll = 256 * t + 16 * (4 * kq + r) + r16;
        const int l = 1024 * role + ll;
        float xv = h2f(*(const u16*)(sx + (2047 - l) * 2));
        float y = rbf(v[r] * kinv);
        so[ll] = (u16)f2bf(y + rbf(xv * dk));
      }
    }
    WAIT_LGKM0;
#pragma unroll
    for (int it = 0; it < 2; ++it) {
      const int c = lane + 64 * it;
      u32x4 v = *(const u32x4*)((char*)so + c * 16);
      __builtin_amdgcn_raw_buffer_store_b128(v, yrs, (d * L + 1024 * role) * 2 + c * 16, 0, 16);
    }
    WAIT_VM(0);
    if (lane == 0) __hip_atomic_store((unsigned*)(ws + WS_YFLAG) + 2 * d + role, 1u, __ATOMIC_RELAXED, __HIP_MEMORY_SCOPE_AGENT);
    STAMP(1,4);
  }
}

extern "C" void kernel_launch(void* const* d_in, const int* in_sizes, int n_in,
                              void* d_out, int out_size, void* d_ws, size_t ws_size,
                              hipStream_t stream) {
  const u16* x = (const u16*)d_in[0]; const u16* lar = (const u16*)d_in[1]; const u16* aim = (const u16*)d_in[2];
  const u16* cr = (const u16*)d_in[3]; const u16* ci = (const u16*)d_in[4]; const u16* ldt = (const u16*)d_in[5];
  const u16* dsk = (const u16*)d_in[6]; const u16* w1 = (const u16*)d_in[8]; const u16* b1 = (const u16*)d_in[9];
  const u16* w2 = (const u16*)d_in[10]; const u16* b2 = (const u16*)d_in[11];
  char* ws = (char*)d_ws;
  kA<<<256, 512, 0, stream>>>(x, lar, aim, cr, ci, ldt, w1, b1, w2, ws);
  kB<<<256, 512, 0, stream>>>(x, dsk, b2, ws, (u16*)d_out);
}
```

```cpp
#include <hip/hip_runtime.h>
#include <stdint.h>

typedef unsigned short u16;
typedef __attribute__((ext_vector_type(8))) __bf16 bf16x8;
typedef __attribute__((ext_vector_type(8))) _Float16 f16x8;
typedef __attribute__((ext_vector_type(4))) short s16x4;
typedef __attribute__((ext_vector_type(4))) float f32x4;
typedef __attribute__((ext_vector_type(4))) unsigned u32x4;
typedef __attribute__((ext_vector_type(2))) unsigned u32x2;
typedef __attribute__((address_space(3))) s16x4* lds_s16x4p;

constexpr int L = 2048, D = 512, NS = 128, F = 1792;
constexpr int XR = 2304;

__device__ __forceinline__ float bf2f(unsigned v) { return __uint_as_float(v << 16); }
__device__ __forceinline__ unsigned f2bf(float f) { __bf16 b = (__bf16)f; return (unsigned)__builtin_bit_cast(u16, b); }
__device__ __forceinline__ float rbf(float f) { return bf2f(f2bf(f)); }
__device__ __forceinline__ unsigned f2h(float f) { _Float16 h = (_Float16)f; return (unsigned)__builtin_bit_cast(u16, h); }
__device__ __forceinline__ float h2f(unsigned v) { return (float)__builtin_bit_cast(_Float16, (u16)v); }

constexpr size_t WS_EPOCH = 0;
constexpr size_t WS_AFLAG = 1024;
constexpr size_t WS_XFLAG = 2048;
constexpr size_t WS_YFLAG = 4096;
constexpr size_t WS_HFLAG = 8192;
constexpr size_t WS_WFLAG = 24576;
constexpr size_t WS_XRT = 32768;
constexpr size_t WS_HT  = WS_XRT + (size_t)D * L * 2;
constexpr size_t WS_YAT = WS_HT + (size_t)L * F * 2;
constexpr size_t WS_W2T = WS_YAT + (size_t)D * L * 2;

typedef __attribute__((ext_vector_type(2))) float f32x2;
__device__ __forceinline__ f32x2 cmul(f32x2 a, f32x2 b) {
  f32x2 t, o;
  asm("v_pk_mul_f32 %0, %1, %2 op_sel:[0,0] op_sel_hi:[0,1]" : "=v"(t) : "v"(a), "v"(b));
  asm("v_pk_fma_f32 %0, %1, %2, %3 op_sel:[1,1,0] op_sel_hi:[1,0,1] neg_lo:[1,0,0]" : "=v"(o) : "v"(a), "v"(b), "v"(t));
  return o;
}
__device__ __forceinline__ f32x2 csq(f32x2 a) { return cmul(a, a); }
__device__ __forceinline__ f32x2 cexp_p(float zr2, float zrev, float p) {
  float e = __builtin_amdgcn_exp2f(zr2 * p);
  float a = __builtin_amdgcn_fractf(zrev * p);
  f32x2 cs = {__builtin_amdgcn_cosf(a), __builtin_amdgcn_sinf(a)};
  return cs * e;
}
__device__ __forceinline__ unsigned pkh(float a, float b) {
  typedef __attribute__((ext_vector_type(2))) _Float16 f16x2;
  f16x2 h = {(_Float16)a, (_Float16)b};
  return __builtin_bit_cast(unsigned, h);
}
__device__ __forceinline__ unsigned pkbf(f32x2 v) {
  typedef __attribute__((ext_vector_type(2))) __bf16 bf16x2;
  bf16x2 h = {(__bf16)v[0], (__bf16)v[1]};
  return __builtin_bit_cast(unsigned, h);
}
__device__ __forceinline__ f32x2 upbf(unsigned v) { return f32x2{__uint_as_float(v << 16), __uint_as_float(v & 0xffff0000u)}; }
#define WAIT_VM(n) asm volatile("s_waitcnt vmcnt(" #n ")" ::: "memory")
#define WAIT_LGKM0 asm volatile("s_waitcnt lgkmcnt(0)" ::: "memory")

__device__ __forceinline__ void lds_arrive(unsigned* cnt, int lane) {
  WAIT_LGKM0;
  if (lane == 0) __hip_atomic_fetch_add(cnt, 1u, __ATOMIC_RELAXED, __HIP_MEMORY_SCOPE_WORKGROUP);
}
__device__ __forceinline__ void lds_wait(unsigned* cnt, unsigned target) {
  while (__hip_atomic_load(cnt, __ATOMIC_RELAXED, __HIP_MEMORY_SCOPE_WORKGROUP) < target) __builtin_amdgcn_s_sleep(1);
  asm volatile("" ::: "memory");
}
__device__ __forceinline__ bf16x8 tr_frag(const char* p) {
  s16x4 lo = __builtin_amdgcn_ds_read_tr16_b64_v4i16((lds_s16x4p)(p));
  s16x4 hi = __builtin_amdgcn_ds_read_tr16_b64_v4i16((lds_s16x4p)(p + 512));
  typedef __attribute__((ext_vector_type(8))) short s16x8;
  s16x8 v = {lo[0], lo[1], lo[2], lo[3], hi[0], hi[1], hi[2], hi[3]};
  return __builtin_bit_cast(bf16x8, v);
}

constexpr int RING = 5;
constexpr int M_XT = 0;
constexpr int M_R1 = 65536;
constexpr int NU = 7;
constexpr int KCS = 2080;
constexpr int CONV_SZ = XR * 2 + 4 * KCS * 2;
constexpr int M_CONV = 0;
constexpr int M_R2 = 43008;
constexpr int M_KP = 157696;
constexpr int M_PB = 147456;
constexpr int M_K2P = 160384;
constexpr int M_FLAG = 160448;
constexpr int M_LDS = M_FLAG + 128;
static_assert(M_LDS <= 163840, "lds");

__device__ __forceinline__ void gls(unsigned voff, const void* sb, unsigned lds_dst) {
  asm volatile("s_mov_b32 m0, %2\n\ts_nop 0\n\tglobal_load_lds_dwordx4 %0, %1" :: "v"(voff), "s"(sb), "s"(lds_dst) : "memory", "m0");
}
__device__ __forceinline__ void gls_sc1(unsigned voff, const void* sb, unsigned lds_dst) {
  asm volatile("s_mov_b32 m0, %2\n\ts_nop 0\n\tglobal_load_lds_dwordx4 %0, %1 sc1" :: "v"(voff), "s"(sb), "s"(lds_dst) : "memory", "m0");
}
__device__ __forceinline__ float red8(float s) {
  s += __builtin_bit_cast(float, __builtin_amdgcn_update_dpp(0, __builtin_bit_cast(int, s), 0xB1, 0xf, 0xf, true));
  s += __builtin_bit_cast(float, __builtin_amdgcn_update_dpp(0, __builtin_bit_cast(int, s), 0x4E, 0xf, 0xf, true));
  s += __builtin_bit_cast(float, __builtin_amdgcn_update_dpp(0, __builtin_bit_cast(int, s), 0x141, 0xf, 0xf, true));
  return s;
}
template <bool EXPORT>
__device__ __forceinline__ void ln_tile(const u32x4 (&v)[8], int w, int lane, char* smem) {
  const int rr = lane >> 3, c = lane & 7;
  f32x2 e[32];
  f32x2 s4[4] = {};
#pragma unroll
  for (int i = 0; i < 8; ++i)
#pragma unroll
    for (int j = 0; j < 4; ++j) { e[4 * i + j] = upbf(v[i][j]); s4[j] += e[4 * i + j]; }
  f32x2 s2 = (s4[0] + s4[1]) + (s4[2] + s4[3]);
  const float mean = red8(s2[0] + s2[1]) * (1.f / D);
  f32x2 qa[4] = {};
#pragma unroll
  for (int i = 0; i < 32; ++i) { f32x2 t = e[i] - mean; qa[i & 3] = __builtin_elementwise_fma(t, t, qa[i & 3]); }
  f32x2 q2 = (qa[0] + qa[1]) + (qa[2] + qa[3]);
  const float q = red8(q2[0] + q2[1]);
  const float m = rbf(mean), vv = rbf(q * (1.f / D));
  const float den = rbf(sqrtf(rbf(vv + 1e-5f)));
  const float rc = 1.0f / den;
  const f32x2 rc2 = {rc, rc}, nden2 = {-den, -den};
  const int j16 = w >> 1, r = 8 * (w & 1) + rr;
#pragma unroll
  for (int i = 0; i < 8; ++i) {
    u32x4 o;
#pragma unroll
    for (int j = 0; j < 4; ++j) {
      unsigned pa = pkbf(e[4 * i + j] - m);
      asm volatile("" : "+v"(pa));
      f32x2 a = upbf(pa);
      f32x2 qq = a * rc2;
      f32x2 rem = __builtin_elementwise_fma(qq, nden2, a);
      qq = __builtin_elementwise_fma(rem, rc2, qq);
      o[j] = pkbf(qq);
      if (EXPORT) { f32x2 u = upbf(o[j]); o[j] = pkh(u[0], u[1]); }
    }
    if (EXPORT) {
      *(u32x4*)(smem + M_XT + (63 - (8 * w + rr)) * 1056 + (64 * i + 8 * c) * 2) = o;
    } else {
      const int ks = 2 * i + (c >> 2), kq = c & 3;
      *(u32x4*)(smem + M_XT + ((((j16 * 16 + ks) * 4 + kq) * 16) + (r ^ (kq | ((ks & 1) << 2)))) * 16) = o;
    }
  }
}
__device__ __forceinline__ u32x2 gelu2(f32x4 accv, u32x2 bv) {
  unsigned o[2];
#pragma unroll
  for (int hh = 0; hh < 2; ++hh) {
    unsigned p0 = pkbf(f32x2{accv[2 * hh], accv[2 * hh + 1]});
    asm volatile("" : "+v"(p0));
    unsigned p1 = pkbf(upbf(p0) + upbf(bv[hh]));
    asm volatile("" : "+v"(p1));
    f32x2 v = upbf(p1);
    f32x2 t = v * v;
    t = __builtin_elementwise_fma(t, f32x2{-0.1029432397f, -0.1029432397f}, f32x2{-2.302208198f, -2.302208198f});
    t = t * v;
    f32x2 ex = {__builtin_amdgcn_exp2f(t[0]), __builtin_amdgcn_exp2f(t[1])};
    ex = ex + 1.0f;
    f32x2 rc = {__builtin_amdgcn_rcpf(ex[0]), __builtin_amdgcn_rcpf(ex[1])};
    o[hh] = pkbf(v * rc);
  }
  return u32x2{o[0], o[1]};
}
template <typename RS>
__device__ __forceinline__ void gelu_store2(f32x4 a0, f32x4 a1, u32x2 bv, RS hrs, int lt16, int f, int lane) {
  u32x2 X = gelu2(a0, bv), Y = gelu2(a1, bv);
  auto s0 = __builtin_amdgcn_permlane16_swap(X[0], Y[0], false, false);
  auto s1 = __builtin_amdgcn_permlane16_swap(X[1], Y[1], false, false);
  u32x4 v = {s0[0], s1[0], s0[1], s1[1]};
  const int kq = lane >> 4, r16 = lane & 15;
  const int off = ((((lt16 + (kq & 1)) * 56 + (f >> 5)) * 64 + ((f >> 3) & 3) * 16 + r16) * 16);
  __builtin_amdgcn_raw_buffer_store_b128(v, hrs, off, 0, 16);
}
#define GSTORE(p, v) __hip_atomic_store((unsigned*)(p), (v), __ATOMIC_RELAXED, __HIP_MEMORY_SCOPE_AGENT)
#define GLOAD(p) __hip_atomic_load((const unsigned*)(p), __ATOMIC_RELAXED, __HIP_MEMORY_SCOPE_AGENT)
constexpr int SPIN_MAX = 1 << 22;

__global__ __launch_bounds__(768, 3) void kM(const u16* __restrict__ x, const u16* __restrict__ lar, const u16* __restrict__ aim,
    const u16* __restrict__ cre, const u16* __restrict__ cim, const u16* __restrict__ ldt, const u16* __restrict__ dsk,
    const u16* __restrict__ w1, const u16* __restrict__ b1, const u16* __restrict__ w2, const u16* __restrict__ b2,
    char* __restrict__ ws, u16* __restrict__ out) {
  __shared__ __attribute__((aligned(16))) char smem[M_LDS];
  const int tid = threadIdx.x, lane = tid & 63, r16 = lane & 15, kq = lane >> 4;
  const int hw = __builtin_amdgcn_readfirstlane(tid >> 6);
  const int b = blockIdx.x;
  unsigned* flags = (unsigned*)(smem + M_FLAG);
  const unsigned sbase = (unsigned)(size_t)smem;
  const bool isg = b < 224;
  const int lt = (b & 7) * 4 + ((b >> 3) & 3), ft = (b >> 3) >> 2;
  const int l0 = lt * 64;
  const int drow = lane >> 3, dsl = lane & 7;
  const int q4 = (lane & 15) >> 2, p4 = lane & 3;
  const int frd = ((q4 >> 1) & 1) | ((kq & 1) << 1);
  u32x4 xv8[8];
  if (hw < 8) {
    const int rr = lane >> 3, c = lane & 7;
#pragma unroll
    for (int i = 0; i < 8; ++i) xv8[i] = *(const u32x4*)(x + (size_t)(l0 + 8 * hw + rr) * D + 64 * i + 8 * c);
  }
  if (tid < 32) flags[tid] = 0;
  __syncthreads();
  if (hw == 7) {
    const unsigned t = GLOAD(ws + WS_EPOCH) + 1u;
    if (lane == 0) { flags[31] = t; GSTORE(ws + WS_AFLAG + b * 4, t); }
    lds_arrive(&flags[30], lane);
  }
  auto get_tok = [&]() { lds_wait(&flags[30], 1); return __hip_atomic_load(&flags[31], __ATOMIC_RELAXED, __HIP_MEMORY_SCOPE_WORKGROUP); };
  auto yrs = __builtin_amdgcn_make_buffer_rsrc((void*)(ws + WS_YAT), 0, D * L * 2, 0x00020000);
  auto hrs = __builtin_amdgcn_make_buffer_rsrc((void*)(ws + WS_HT), 0, L * F * 2, 0x00020000);
  auto xrs = __builtin_amdgcn_make_buffer_rsrc((void*)(ws + WS_XRT), 0, D * L * 2, 0x00020000);
  if (hw >= 8) {
    const int w = hw - 8;
    unsigned aoff[2];
#pragma unroll
    for (int qq = 0; qq < 2; ++qq) {
      int fr = ((drow >> 1) & 1) | (qq << 1);
      aoff[qq] = (drow * D + (((dsl >> 1) ^ fr) * 16 + (dsl & 1) * 8)) * 2;
    }
    const int tid2 = b * 4 + w;
    const bool dot2 = tid2 < 448;
    if (dot2) {
#pragma unroll
      for (int q = 0; q < 4; ++q)
        gls(aoff[q & 1], w2 + (size_t)(32 * (tid2 >> 3) + 8 * q) * D + (tid2 & 7) * 64, sbase + M_R1 + w * (RING * 4096) + 4 * 4096 + q * 1024);
    }
    auto wrs = __builtin_amdgcn_make_buffer_rsrc((void*)(ws + WS_W2T), 0, F * D * 2, 0x00020000);
    auto tile_w2 = [&]() {
      if (dot2) {
        WAIT_VM(0);
#pragma unroll
        for (int i = 0; i < 4; ++i) {
          bf16x8 v = tr_frag(smem + M_R1 + w * (RING * 4096) + 4 * 4096 + (8 * kq + q4) * 128 + ((i ^ frd) * 32) + p4 * 8);
          __builtin_amdgcn_raw_buffer_store_b128(__builtin_bit_cast(u32x4, v), wrs, ((((tid2 & 7) * 4 + i) * 56 + (tid2 >> 3)) * 64 + lane) * 16, 0, 16);
        }
        WAIT_VM(0);
        const unsigned t = get_tok();
        if (lane == 0) GSTORE(ws + WS_WFLAG + ((tid2 & 7) * 64 + (tid2 >> 3)) * 4, t);
      }
    };
    if (isg) {
      const int fw0 = ft * 256 + w * 64;
      const unsigned ring = sbase + M_R1 + w * (RING * 4096);
      unsigned woff[2];
#pragma unroll
      for (int qq = 0; qq < 2; ++qq) {
        int fr = ((drow >> 1) & 1) | (qq << 1);
        woff[qq] = (drow * F + (((dsl >> 1) ^ fr) * 16 + (dsl & 1) * 8)) * 2;
      }
      const u16* wb = w1 + fw0;
      auto issue = [&](int s) {
#pragma unroll
        for (int q = 0; q < 4; ++q) gls(woff[q & 1], wb + (size_t)(32 * s + 8 * q) * F, ring + (s % RING) * 4096 + q * 1024);
      };
#pragma unroll
      for (int s = 0; s < RING - 1; ++s) issue(s);
      const u32x2 bv0 = *(const u32x2*)(b1 + fw0 + 4 * kq), bv1 = *(const u32x2*)(b1 + fw0 + 16 + 4 * kq), bv2 = *(const u32x2*)(b1 + fw0 + 32 + 4 * kq);
      tile_w2();
      lds_wait(&flags[0], 8);
      const char* ap[4];
#pragma unroll
      for (int i = 0; i < 4; ++i) ap[i] = smem + M_R1 + w * (RING * 4096) + (8 * kq + q4) * 128 + ((i ^ frd) * 32) + p4 * 8;
      const char* bp0 = smem + M_XT + (kq * 16 + (r16 ^ kq)) * 16;
      const char* bp1 = smem + M_XT + (kq * 16 + (r16 ^ (kq | 4))) * 16;
      f32x4 acc[4][4] = {};
      bf16x8 a[2][4], bb[2][4];
      auto rdfr = [&](int s, bf16x8 (&aa)[4], bf16x8 (&bq)[4]) {
#pragma unroll
        for (int i = 0; i < 4; ++i) aa[i] = tr_frag(ap[i] + (s % RING) * 4096);
#pragma unroll
        for (int j = 0; j < 4; ++j) bq[j] = *(const bf16x8*)(((s & 1) ? bp1 : bp0) + (j * 16 + s) * 1024);
      };
      WAIT_VM(12);
      rdfr(0, a[0], bb[0]);
#pragma unroll
      for (int s = 0; s < 16; ++s) {
        if (s + RING - 1 < 16) { issue(s + RING - 1); WAIT_VM(12); }
        else if (s + RING - 1 == 16) { WAIT_VM(8); }
        else if (s + RING - 1 == 17) { WAIT_VM(4); }
        else { WAIT_VM(0); }
        if (s + 1 < 16) rdfr(s + 1, a[(s + 1) & 1], bb[(s + 1) & 1]);
#pragma unroll
        for (int i = 0; i < 4; ++i)
#pragma unroll
          for (int j = 0; j < 4; ++j) acc[i][j] = __builtin_amdgcn_mfma_f32_16x16x32_bf16(a[s & 1][i], bb[s & 1][j], acc[i][j], 0, 0, 0);
      }
      {
        f32x4* tx = (f32x4*)(smem + M_R1 + w * (RING * 4096));
#pragma unroll
        for (int t = 10; t < 16; ++t) tx[(t - 8) * 64 + lane] = acc[t >> 2][t & 3];
        lds_arrive(&flags[4 + w], lane);
        lds_arrive(&flags[24], lane);
      }
      gelu_store2(acc[0][0], acc[0][1], bv0, hrs, (l0 >> 4), fw0 + 4 * kq, lane);
      gelu_store2(acc[0][2], acc[0][3], bv0, hrs, (l0 >> 4) + 2, fw0 + 4 * kq, lane);
      gelu_store2(acc[1][0], acc[1][1], bv1, hrs, (l0 >> 4), fw0 + 16 + 4 * kq, lane);
      gelu_store2(acc[1][2], acc[1][3], bv1, hrs, (l0 >> 4) + 2, fw0 + 16 + 4 * kq, lane);
      gelu_store2(acc[2][0], acc[2][1], bv2, hrs, (l0 >> 4), fw0 + 32 + 4 * kq, lane);
      WAIT_VM(0);
    } else {
      tile_w2();
      lds_arrive(&flags[24], lane);
    }
    const unsigned tok = get_tok();
    if (isg && lane == 0) GSTORE(ws + WS_HFLAG + ((lt * 8 + ft) * 16 + 4 + w) * 4, tok);
    lds_arrive(&flags[1], lane);
    lds_wait(&flags[1], 12);
    __builtin_amdgcn_s_setprio(2);
    const int d0 = ft * 64;
    const char* Ap = ws + WS_W2T + (((size_t)(ft * 4) * 56 + w * 14) * 64) * 16;
    const char* Bp = ws + WS_HT + (((size_t)(lt * 4) * 56 + w * 14) * 64) * 16;
    const unsigned boff = lane * 16;
    const unsigned ring = sbase + M_R2 + w * (NU * 4096);
    auto issue2 = [&](int u) {
#pragma unroll
      for (int i = 0; i < 4; ++i) {
        if (u & 1) gls_sc1(boff, Bp + i * (56 * 1024) + (u >> 1) * 1024, ring + (u % NU) * 4096 + i * 1024);
        else gls_sc1(boff, Ap + i * (56 * 1024) + (u >> 1) * 1024, ring + (u % NU) * 4096 + i * 1024);
      }
    };
    {
      const char* fp = ws + WS_WFLAG + (ft * 64 + w * 14 + (lane < 14 ? lane : 0)) * 4;
      for (int spin = 0; spin < SPIN_MAX; ++spin) {
        if (__all(GLOAD(fp) == tok)) break;
        __builtin_amdgcn_s_sleep(2);
      }
      asm volatile("" ::: "memory");
    }
    issue2(0); issue2(2); issue2(4); issue2(6);
    const int l = l0 + 16 * w + r16;
    u32x2 xv[4], bv[4];
#pragma unroll
    for (int i = 0; i < 4; ++i) { xv[i] = *(const u32x2*)(x + (size_t)l * D + d0 + 16 * i + 4 * kq); bv[i] = *(const u32x2*)(b2 + d0 + 16 * i + 4 * kq); }
    {
      const int i0 = lane < 14 ? lane : 0;
      const int grp = 7 * w + (i0 >> 1);
      const char* fp0 = ws + WS_HFLAG + ((lt * 8 + (grp >> 2)) * 16 + (grp & 3) + ((i0 & 1) ? 0 : 4)) * 4;
      for (int spin = 0; spin < SPIN_MAX; ++spin) {
        if (__all(GLOAD(fp0) == tok)) break;
        __builtin_amdgcn_s_sleep(2);
      }
      asm volatile("" ::: "memory");
    }
    issue2(1); issue2(3); issue2(5);
#pragma unroll
    for (int i = 0; i < 4; ++i) asm volatile("" : "+v"(xv[i]), "+v"(bv[i]));
    WAIT_VM(0);
    const char* rp = smem + M_R2 + w * (NU * 4096) + lane * 16;
    f32x4 acc[4][4] = {};
    bf16x8 a[2][4], bb[2][4];
    auto rdfr2 = [&](int s, bf16x8 (&aa)[4], bf16x8 (&bq)[4]) {
#pragma unroll
      for (int i = 0; i < 4; ++i) aa[i] = *(const bf16x8*)(rp + ((2 * s) % NU) * 4096 + i * 1024);
#pragma unroll
      for (int j = 0; j < 4; ++j) bq[j] = *(const bf16x8*)(rp + ((2 * s + 1) % NU) * 4096 + j * 1024);
    };
    rdfr2(0, a[0], bb[0]);
#pragma unroll
    for (int s = 0; s < 14; ++s) {
      WAIT_LGKM0;
      if (2 * s + 7 < 28) issue2(2 * s + 7);
      if (2 * s + 8 < 28) issue2(2 * s + 8);
      if (2 * s + 8 <= 27) { WAIT_VM(20); }
      else if (s == 10) { WAIT_VM(16); }
      else if (s == 11) { WAIT_VM(8); }
      else { WAIT_VM(0); }
      if (s + 1 < 14) rdfr2(s + 1, a[(s + 1) & 1], bb[(s + 1) & 1]);
#pragma unroll
      for (int i = 0; i < 4; ++i)
#pragma unroll
        for (int j = 0; j < 4; ++j) acc[i][j] = __builtin_amdgcn_mfma_f32_16x16x32_bf16(a[s & 1][i], bb[s & 1][j], acc[i][j], 0, 0, 0);
    }
    {
      f32x4* mine = (f32x4*)(smem + M_R2 + w * (NU * 4096));
#pragma unroll
      for (int j = 0; j < 4; ++j) {
        if (j != w) {
          const int slot = (j > w) ? j - 1 : j;
#pragma unroll
          for (int i = 0; i < 4; ++i) mine[(slot * 4 + i) * 64 + lane] = acc[i][j];
        }
      }
    }
    lds_arrive(&flags[2], lane);
    {
      const unsigned long long* fp = (const unsigned long long*)(ws + WS_YFLAG) + d0 + lane;
      const unsigned long long want = ((unsigned long long)tok << 32) | tok;
      for (int spin = 0; spin < SPIN_MAX; ++spin) {
        unsigned long long v = __hip_atomic_load(fp, __ATOMIC_RELAXED, __HIP_MEMORY_SCOPE_AGENT);
        if (__all(v == want)) break;
        __builtin_amdgcn_s_sleep(4);
      }
      asm volatile("" ::: "memory");
    }
    unsigned yv[4][4];
#pragma unroll
    for (int i = 0; i < 4; ++i)
#pragma unroll
      for (int r = 0; r < 4; ++r)
        yv[i][r] = __builtin_amdgcn_raw_buffer_load_b16(yrs, ((d0 + 16 * i + 4 * kq + r) * L + l) * 2, 0, 16);
    lds_wait(&flags[2], 4);
#pragma unroll
    for (int j = 0; j < 4; ++j) {
      if (j == w) {
#pragma unroll
        for (int i = 0; i < 4; ++i) {
          f32x4 v = acc[i][j];
#pragma unroll
          for (int src = 0; src < 4; ++src)
            if (src != j) v += ((const f32x4*)(smem + M_R2 + src * (NU * 4096)))[(((j > src) ? j - 1 : j) * 4 + i) * 64 + lane];
          unsigned o[2];
#pragma unroll
          for (int hh = 0; hh < 2; ++hh) {
            f32x2 m2 = upbf(pkbf(upbf(pkbf(f32x2{v[2 * hh], v[2 * hh + 1]})) + upbf(bv[i][hh])));
            f32x2 ya = {bf2f(yv[i][2 * hh] & 0xffff), bf2f(yv[i][2 * hh + 1] & 0xffff)};
            o[hh] = pkbf(upbf(pkbf(upbf(xv[i][hh]) + ya)) + m2);
          }
          *(u32x2*)(out + (size_t)l * D + d0 + 16 * i + 4 * kq) = u32x2{o[0], o[1]};
        }
      }
    }
  } else {
    const int aw = hw, pr = aw >> 2, role = aw & 3;
    const int d = 2 * b + pr;
    __builtin_amdgcn_s_setprio(3);
    const int n = role * 32 + (lane & 31);
    const unsigned p_ldt = ldt[d], p_lar = lar[d * NS + n], p_aim = aim[d * NS + n], p_cr = cre[d * NS + n], p_ci = cim[d * NS + n];
    const unsigned p_c0 = ((const unsigned*)cre)[d * (NS / 2) + lane], p_c1 = ((const unsigned*)cim)[d * (NS / 2) + lane];
    const unsigned p_dk = dsk[d];
    const int gw = pr * 2 + (role & 1);
    const int hf0 = ft * 256 + gw * 64;
    u32x2 hb[2];
    hb[0] = *(const u32x2*)(b1 + (isg ? hf0 : 0) + 32 + 4 * kq);
    hb[1] = *(const u32x2*)(b1 + (isg ? hf0 : 0) + 48 + 4 * kq);
    ln_tile<false>(xv8, aw, lane, smem);
    lds_arrive(&flags[0], lane);
    if (pr == 0) __builtin_amdgcn_s_setprio(1); else __builtin_amdgcn_s_setprio(2);
    const float dt = rbf(expf(bf2f(p_ldt)));
    const float ea = rbf(expf(bf2f(p_lar)));
    const float ar = -ea * dt, ai = bf2f(p_aim) * dt;
    const float Cr = bf2f(p_cr) * dt, Ci = bf2f(p_ci) * dt;
    float ssum = fabsf(bf2f(p_c0 & 0xffff) * dt) + fabsf(bf2f(p_c0 >> 16) * dt) + fabsf(bf2f(p_c1 & 0xffff) * dt) + fabsf(bf2f(p_c1 >> 16) * dt);
#pragma unroll
    for (int o = 32; o; o >>= 1) ssum += __shfl_xor(ssum, o);
    const float B2 = 2.f * ssum;
    int e = 0;
    if (B2 > 0.f && B2 < 3.0e38f) { (void)frexpf(B2, &e); }
    int se = 14 - e; se = se > 120 ? 120 : (se < -120 ? -120 : se);
    const float scale = __uint_as_float((unsigned)(se + 127) << 23);
    const float zr2 = ar * 1.4426950408889634f, zrev = ai * 0.15915494309189535f;
    const float C0r = Cr * (2.f * scale), C0i = Ci * (2.f * scale);
    float k2 = cmul(f32x2{C0r, C0i}, cexp_p(zr2, zrev, 2048.f))[0];
#pragma unroll
    for (int o = 16; o; o >>= 1) k2 += __shfl_xor(k2, o);
    const unsigned tok = get_tok();
    lds_wait(&flags[0], 8);
    {
      const int task = aw * 64 + lane;
      const int dd = ft * 64 + (task >> 3), m = task & 7;
      const int ks = dd >> 5, kqd = (dd >> 3) & 3, hx = kqd | ((ks & 1) << 2);
      unsigned o[4];
#pragma unroll
      for (int e2 = 0; e2 < 4; ++e2) {
        float v2[2];
#pragma unroll
        for (int h = 0; h < 2; ++h) {
          const int rl = 63 - 8 * m - (2 * e2 + h);
          v2[h] = bf2f(*(const u16*)(smem + M_XT + (((((rl >> 4) * 16 + ks) * 4 + kqd) * 16) + ((rl & 15) ^ hx)) * 16 + (dd & 7) * 2));
        }
        o[e2] = pkh(v2[0], v2[1]);
      }
      __builtin_amdgcn_raw_buffer_store_b128(u32x4{o[0], o[1], o[2], o[3]}, xrs, (dd * L + (1984 - l0) + 8 * m) * 2, 0, 16);
    }
    WAIT_VM(0);
    lds_arrive(&flags[3], lane);
    if (aw == 7) { lds_wait(&flags[3], 8); if (lane == 0) GSTORE(ws + WS_XFLAG + (ft * 32 + lt) * 4, tok); }
    char* pb0 = smem + M_PB + pr * 5120;
    char* pb1 = pb0 + 2560;
    float* k2p = (float*)(smem + M_K2P + pr * 16);
    u16* kpw = (u16*)(smem + M_KP + pr * 1344);
    unsigned* flk = flags + 8 + 4 * pr;
    if (lane == 0) k2p[role] = k2;
    const float tf = (float)r16;
    f32x4 kacc[4] = {};
#pragma unroll
    for (int ss = 0; ss < 2; ++ss) {
      u32x4 A[4], Bv[4];
#pragma unroll
      for (int mm = 0; mm < 4; ++mm) {
        const int sl = (16 * ss + 4 * kq + mm) * 4;
        const float c0 = __builtin_bit_cast(float, __builtin_amdgcn_ds_bpermute(sl, __builtin_bit_cast(int, zr2)));
        const float c1 = __builtin_bit_cast(float, __builtin_amdgcn_ds_bpermute(sl, __builtin_bit_cast(int, zrev)));
        const float c2 = __builtin_bit_cast(float, __builtin_amdgcn_ds_bpermute(sl, __builtin_bit_cast(int, C0r)));
        const float c3 = __builtin_bit_cast(float, __builtin_amdgcn_ds_bpermute(sl, __builtin_bit_cast(int, C0i)));
        f32x2 e1 = cexp_p(c0, c1, tf);
        f32x2 e2 = csq(e1), e4 = csq(e2), e8 = csq(e4), e16 = csq(e8), e32 = csq(e16), e64 = csq(e32);
        f32x2 G0 = cmul(f32x2{c2, c3}, e16), G1 = cmul(G0, e16), G2 = cmul(G1, e32), G3 = cmul(G2, e64);
        A[0][mm] = pkh(G0[0], -G0[1]); A[1][mm] = pkh(G1[0], -G1[1]); A[2][mm] = pkh(G2[0], -G2[1]); A[3][mm] = pkh(G3[0], -G3[1]);
        Bv[0][mm] = pkh(e1[0], e1[1]); Bv[1][mm] = pkh(e2[0], e2[1]); Bv[2][mm] = pkh(e4[0], e4[1]); Bv[3][mm] = pkh(e8[0], e8[1]);
      }
#pragma unroll
      for (int r = 0; r < 4; ++r)
        kacc[r] = __builtin_amdgcn_mfma_f32_16x16x32_f16(__builtin_bit_cast(f16x8, A[r]), __builtin_bit_cast(f16x8, Bv[r]), kacc[r], 0, 0, 0);
    }
    auto pofs = [&](int r) { return r == 0 ? lane * 16 : 1024 + (r - 1) * 512 + (lane & 31) * 16; };
    if (role & 1) {
      char* pb = role == 1 ? pb0 : pb1;
#pragma unroll
      for (int r = 0; r < 4; ++r) if (r == 0 || lane >= 32) *(f32x4*)(pb + pofs(r)) = kacc[r];
      lds_arrive(&flk[role >> 1], lane);
    } else {
      lds_wait(&flk[role >> 1], 1);
      char* pb = role == 0 ? pb0 : pb1;
#pragma unroll
      for (int r = 0; r < 4; ++r) kacc[r] += *(const f32x4*)(pb + pofs(r));
      if (role == 2) {
#pragma unroll
        for (int r = 0; r < 4; ++r) if (r == 0 || lane >= 32) *(f32x4*)(pb + pofs(r)) = kacc[r];
        lds_arrive(&flk[2], lane);
      } else {
        lds_wait(&flk[2], 1);
        const float k2s = (k2p[0] + k2p[1]) + (k2p[2] + k2p[3]);
#pragma unroll
        for (int r = 0; r < 4; ++r) {
          f32x4 v = kacc[r] + *(const f32x4*)(pb1 + pofs(r));
          if (r == 0 || lane >= 32) {
#pragma unroll
            for (int j = 0; j < 4; ++j) kpw[128 * r + 16 * (4 * kq + j) + r16] = (u16)f2h(v[j]);
          }
        }
        if (lane == 0) { kpw[640] = (u16)f2h(k2s); *(float*)(kpw + 644) = __uint_as_float((unsigned)(127 - se) << 23); }
        lds_arrive(&flk[3], lane);
      }
    }
    if (isg && role >= 2) {
      lds_wait(&flags[4 + gw], 1);
      const f32x4* tx = (const f32x4*)(smem + M_R1 + gw * (RING * 4096));
      gelu_store2(tx[2 * 64 + lane], tx[3 * 64 + lane], hb[0], hrs, (l0 >> 4) + 2, hf0 + 32 + 4 * kq, lane);
      gelu_store2(tx[4 * 64 + lane], tx[5 * 64 + lane], hb[1], hrs, (l0 >> 4), hf0 + 48 + 4 * kq, lane);
      gelu_store2(tx[6 * 64 + lane], tx[7 * 64 + lane], hb[1], hrs, (l0 >> 4) + 2, hf0 + 48 + 4 * kq, lane);
      lds_arrive(&flags[1], lane);
      WAIT_VM(0);
      if (lane == 0) GSTORE(ws + WS_HFLAG + ((lt * 8 + ft) * 16 + gw) * 4, tok);
    } else {
      lds_arrive(&flags[1], lane);
    }
    if (role < 2) {
      const int cr = role;
      {
        const char* fp = ws + WS_XFLAG + ((d >> 6) * 32 + (lane & 31)) * 4;
        for (int spin = 0; spin < SPIN_MAX; ++spin) {
          if (__all(GLOAD(fp) == tok)) break;
          __builtin_amdgcn_s_sleep(8);
        }
        asm volatile("" ::: "memory");
      }
      u32x4 xa[2];
#pragma unroll
      for (int it = 0; it < 2; ++it) xa[it] = __builtin_amdgcn_raw_buffer_load_b128(xrs, (d * L) * 2 + (cr * 128 + it * 64 + lane) * 16, 0, 16);
      __builtin_amdgcn_s_setprio(3);
      char* sx = smem + M_CONV + pr * CONV_SZ;
      char* sk = sx + XR * 2;
      const u16* kp = (const u16*)(smem + M_KP + pr * 1344);
      unsigned* fl = flags + 16 + 4 * pr;
      lds_wait(&flags[24], 4);
      lds_wait(&flags[3], 8);
      lds_wait(&flags[8 + 4 * pr + 3], 1);
      const float kinv = *(const float*)(kp + 644);
#pragma unroll
      for (int it = 0; it < 2; ++it) {
        const int gl = cr * 128 + it * 64 + lane;
        unsigned kv[6];
#pragma unroll
        for (int j = 0; j < 6; ++j) {
          unsigned v2[2];
#pragma unroll
          for (int q = 0; q < 2; ++q) {
            int l = 8 * gl + 2 * j + q;
            int idx = l < 128 ? l : (int)f2bf((float)l) - 17024;
            idx = idx > 640 ? 640 : idx;
            v2[q] = kp[idx];
          }
          kv[j] = v2[0] | (v2[1] << 16);
        }
        const int g = gl + 2;
        *(u32x4*)(sk + 0 * KCS * 2 + g * 16) = u32x4{kv[0], kv[1], kv[2], kv[3]};
        *(u32x4*)(sk + 1 * KCS * 2 + g * 16) = u32x4{__builtin_amdgcn_alignbit(kv[1], kv[0], 16), __builtin_amdgcn_alignbit(kv[2], kv[1], 16),
                                                    __builtin_amdgcn_alignbit(kv[3], kv[2], 16), __builtin_amdgcn_alignbit(kv[4], kv[3], 16)};
        *(u32x4*)(sk + 2 * KCS * 2 + g * 16) = u32x4{kv[1], kv[2], kv[3], kv[4]};
        *(u32x4*)(sk + 3 * KCS * 2 + g * 16) = u32x4{__builtin_amdgcn_alignbit(kv[2], kv[1], 16), __builtin_amdgcn_alignbit(kv[3], kv[2], 16),
                                                    __builtin_amdgcn_alignbit(kv[4], kv[3], 16), __builtin_amdgcn_alignbit(kv[5], kv[4], 16)};
        if (gl < 4) *(u32x4*)(sk + gl * KCS * 2) = u32x4{0, 0, 0, 0};
        if (gl == 0) {
          *(u32x4*)(sk + 0 * KCS * 2 + 16) = u32x4{0, 0, 0, 0};
          *(u32x4*)(sk + 1 * KCS * 2 + 16) = u32x4{0, 0, 0, kv[0] << 16};
          *(u32x4*)(sk + 2 * KCS * 2 + 16) = u32x4{0, 0, 0, kv[0]};
          *(u32x4*)(sk + 3 * KCS * 2 + 16) = u32x4{0, 0, kv[0] << 16, __builtin_amdgcn_alignbit(kv[1], kv[0], 16)};
        }
        *(u32x4*)(sx + gl * 16) = xa[it];
        if (gl < 32) *(u32x4*)(sx + (256 + gl) * 16) = u32x4{0, 0, 0, 0};
      }
      lds_arrive(&fl[0], lane);
      lds_wait(&fl[0], 2);
      f32x4 acc[8] = {};
      const char* pa = sx + (2032 - 16 * r16 + 8 * kq + 128 * cr) * 2;
      const int n1 = r16 + 1;
      const char* pb = sk + (n1 & 3) * KCS * 2 + (8 * kq + (n1 & ~3) + 128 * cr) * 2;
#pragma unroll 1
      for (int rho = 0; rho < 4; ++rho) {
        f16x8 a[8], bq[8];
#pragma unroll
        for (int tp = 0; tp < 8; ++tp) a[tp] = *(const f16x8*)(pa + (32 * (rho - 8 * tp)) * 2);
#pragma unroll
        for (int sg = 0; sg < 8; ++sg) {
          const char* p = pb + (32 * (8 * sg + rho)) * 2;
          u32x2 lo = *(const u32x2*)p, hi = *(const u32x2*)(p + 8);
          u32x4 v = {lo[0], lo[1], hi[0], hi[1]};
          bq[sg] = __builtin_bit_cast(f16x8, v);
        }
#pragma unroll
        for (int sg = 0; sg < 8; ++sg)
#pragma unroll
          for (int tp = 0; tp < 8 - sg; ++tp)
            acc[sg + tp] = __builtin_amdgcn_mfma_f32_16x16x32_f16(a[tp], bq[sg], acc[sg + tp], 0, 0, 0);
      }
      lds_arrive(&fl[1], lane);
      lds_wait(&fl[1], 2);
      f32x4* cx = (f32x4*)sk;
      f32x4 keep[4];
      if (cr) {
#pragma unroll
        for (int t = 0; t < 4; ++t) { cx[(4 + t) * 64 + lane] = acc[t]; keep[t] = acc[4 + t]; }
      } else {
#pragma unroll
        for (int t = 0; t < 4; ++t) { cx[t * 64 + lane] = acc[4 + t]; keep[t] = acc[t]; }
      }
      lds_arrive(&fl[2], lane);
      lds_wait(&fl[2], 2);
      const float dk = bf2f(p_dk);
      u16* so = (u16*)(sk + 8192 + cr * 2048);
#pragma unroll
      for (int t = 0; t < 4; ++t) {
        f32x4 v = keep[t] + cx[((cr ^ 1) * 4 + t) * 64 + lane];
#pragma unroll
        for (int r = 0; r < 4; ++r) {
          const int ll = 256 * t + 16 * (4 * kq + r) + r16;
          const int l = 1024 * cr + ll;
          float xvv = h2f(*(const u16*)(sx + (2047 - l) * 2));
          float y = rbf(v[r] * kinv);
          so[ll] = (u16)f2bf(y + rbf(xvv * dk));
        }
      }
      WAIT_LGKM0;
#pragma unroll
      for (int it = 0; it < 2; ++it) {
        const int c = lane + 64 * it;
        u32x4 v = *(const u32x4*)((char*)so + c * 16);
        __builtin_amdgcn_raw_buffer_store_b128(v, yrs, (d * L + 1024 * cr) * 2 + c * 16, 0, 16);
      }
      WAIT_VM(0);
      if (lane == 0) GSTORE(ws + WS_YFLAG + (2 * d + cr) * 4, tok);
    } else if (b == 0 && aw == 2) {
      for (int spin = 0; spin < SPIN_MAX; ++spin) {
        bool ok = true;
#pragma unroll
        for (int i = 0; i < 4; ++i) ok = ok && (GLOAD(ws + WS_AFLAG + (i * 64 + lane) * 4) == tok);
        if (__all(ok)) break;
        __builtin_amdgcn_s_sleep(8);
      }
      if (lane == 0) GSTORE(ws + WS_EPOCH, tok);
    }
  }
}

extern "C" void kernel_launch(void* const* d_in, const int* in_sizes, int n_in,
                              void* d_out, int out_size, void* d_ws, size_t ws_size,
                              hipStream_t stream) {
  const u16* x = (const u16*)d_in[0]; const u16* lar = (const u16*)d_in[1]; const u16* aim = (const u16*)d_in[2];
  const u16* cr = (const u16*)d_in[3]; const u16* ci = (const u16*)d_in[4]; const u16* ldt = (const u16*)d_in[5];
  const u16* dsk = (const u16*)d_in[6]; const u16* w1 = (const u16*)d_in[8]; const u16* b1 = (const u16*)d_in[9];
  const u16* w2 = (const u16*)d_in[10]; const u16* b2 = (const u16*)d_in[11];
  kM<<<256, 768, 0, stream>>>(x, lar, aim, cr, ci, ldt, dsk, w1, b1, w2, b2, (char*)d_ws, (u16*)d_out);
}
```
